```python
import torch

def _softmax(x, axis=-1):
    return torch.softmax(x, dim=axis)
def _gelu(x):
    return torch.nn.functional.gelu(x, approximate="tanh")
def _silu(x):
    return torch.nn.functional.silu(x)
def _sigmoid(x):
    return torch.sigmoid(x)
def _layernorm(x, axis=-1, eps=1e-5):
    m = x.mean(dim=axis, keepdim=True)
    v = x.var(dim=axis, keepdim=True, unbiased=False)
    return (x - m) / torch.sqrt(v + eps)
def _scatter_add(tgt, idx, src, axis=0):
    return tgt.clone().index_add_(axis, idx, src)
def _segment_sum(src, seg, n):
    out = torch.zeros((n,) + src.shape[1:], dtype=src.dtype)
    return out.index_add_(0, seg, src)
def _pad(x, pw):
    flat = tuple(v for pair in reversed(pw) for v in pair)
    return torch.nn.functional.pad(x, flat)

kernel_name = 'recurrent_scan__mha_block_cached__dense_activation_gelu__decode'

def setup_inputs():

    _w = (lambda a: (lambda t: t.to(torch.bfloat16) if t.is_floating_point() else t)(torch.as_tensor(a)))
    def _A_setup():
        B, Sq, D = 1, 1, 640
        H, Sk, Dh = 32, 32768, 192
        return {
            "x": _w((lambda *s: torch.randn(*s, dtype=torch.bfloat16) * 0.4)(B, Sq, D)),
            "k_cache": _w((lambda *s: torch.randn(*s, dtype=torch.bfloat16) * 1.6)(B, H, Sk, Dh)),
            "v_cache": _w((lambda *s: torch.randn(*s, dtype=torch.bfloat16) * 1.6)(B, H, Sk, Dh)),
            "wq": _w((lambda *s: torch.randn(*s, dtype=torch.bfloat16) * 1.6)(D, H * Dh) * (D ** -0.5)),
            "wo": _w((lambda *s: torch.randn(*s, dtype=torch.bfloat16) * 1.6)(H * Dh, D) * ((H * Dh) ** -0.5)),
        }
    _A_in = _A_setup()

    def _B_setup():
        B, S, D, F = 1, 1, 640, 1536
        return {
            "x": _w((lambda *s: torch.randn(*s, dtype=torch.bfloat16) * 0.4)(B, S, D)),
            "w1": _w((lambda *s: torch.randn(*s, dtype=torch.bfloat16) * 1.6)(D, F) * (D ** -0.5)),
            "b1": _w((lambda *s: torch.randn(*s, dtype=torch.bfloat16) * 1.6)(F) * 0.01),
            "w2": _w((lambda *s: torch.randn(*s, dtype=torch.bfloat16) * 1.6)(F, D) * (F ** -0.5)),
            "b2": _w((lambda *s: torch.randn(*s, dtype=torch.bfloat16) * 1.6)(D) * 0.01),
        }
    _B_in = _B_setup()

    return {
        "A_x": _A_in["x"],
        "A_k_cache": _A_in["k_cache"],
        "A_v_cache": _A_in["v_cache"],
        "A_wq": _A_in["wq"],
        "A_wo": _A_in["wo"],
        "B_x": _B_in["x"],
        "B_w1": _B_in["w1"],
        "B_b1": _B_in["b1"],
        "B_w2": _B_in["w2"],
        "B_b2": _B_in["b2"],
    }

def reference(A_x, A_k_cache, A_v_cache, A_wq, A_wo, B_x, B_w1, B_b1, B_w2, B_b2):
    def _A_ref(x, k_cache, v_cache, wq, wo):
        B, Sq, D = x.shape
        H, Dh = 32, 192
        q = (lambda x, *d: x.permute(*d))((x @ wq).reshape(B, Sq, H, Dh), 0, 2, 1, 3)
        scores = torch.einsum("bhqd,bhkd->bhqk", q, k_cache) * (Dh ** -0.5)
        p = _softmax(scores, axis=-1)
        o = torch.einsum("bhqk,bhkd->bhqd", p, v_cache)
        return (lambda x, *d: x.permute(*d))(o, 0, 2, 1, 3).reshape(B, Sq, H * Dh) @ wo

    def _B_ref(x, w1, b1, w2, b2):
        h = torch.einsum("bsd,df->bsf", x, w1) + b1
        h = _gelu(h)
        return torch.einsum("bsf,fd->bsd", h, w2) + b2

    x = A_x
    xn = _layernorm(x, axis=-1)
    s = _A_ref(xn, A_k_cache, A_v_cache, A_wq, A_wo)
    g = _silu(_B_ref(xn, B_w1, B_b1, B_w2, B_b2))
    return x + s * g
```

```cpp
#include <hip/hip_runtime.h>
#include <hip/hip_bf16.h>
#include <stdint.h>

constexpr int DM = 640, NH = 32, SK = 32768, DH = 192, FF = 1536, HD = NH * DH;
#define NWAVES 8
#define NSPLIT 16
#define TSTEP 2
#define MIMIC 0
constexpr int KPW = SK / NSPLIT;
constexpr int TILE_ROWS = 8 * TSTEP;
constexpr int NTILES = KPW / (TILE_ROWS * NWAVES);
static_assert(KPW % (TILE_ROWS * NWAVES) == 0, "");
constexpr int PREC = 200;

constexpr size_t WS_Q = 0;
constexpr size_t WS_H = 16384;
constexpr size_t WS_PART = 65536;
constexpr size_t WS_Y = WS_PART + (size_t)NH * 64 * PREC * 4;
constexpr size_t WS_Z = WS_Y + (size_t)256 * DM * 4;

typedef unsigned u32x4 __attribute__((ext_vector_type(4)));
typedef float f32x4 __attribute__((ext_vector_type(4)));
typedef float f32x2 __attribute__((ext_vector_type(2)));
typedef __bf16 bf16x2 __attribute__((ext_vector_type(2)));

__device__ __forceinline__ float bf2f(unsigned short u) { return __builtin_bit_cast(float, (unsigned)u << 16); }
__device__ __forceinline__ float bflo(unsigned u) { return __builtin_bit_cast(float, u << 16); }
__device__ __forceinline__ float bfhi(unsigned u) { return __builtin_bit_cast(float, u & 0xffff0000u); }
__device__ __forceinline__ float rbf(float x) { return (float)(__bf16)x; }
__device__ __forceinline__ unsigned short f2bf(float x) { return __builtin_bit_cast(unsigned short, (__bf16)x); }
__device__ __forceinline__ float dot2(unsigned k, unsigned q, float acc) {
  return __builtin_amdgcn_fdot2_f32_bf16(__builtin_bit_cast(bf16x2, k), __builtin_bit_cast(bf16x2, q), acc, false);
}
template <int CTRL> __device__ __forceinline__ float dpp(float x) {
  return __builtin_bit_cast(float, __builtin_amdgcn_mov_dpp(__builtin_bit_cast(int, x), CTRL, 0xf, 0xf, true));
}
constexpr int XOR1 = 0xB1, XOR2 = 0x4E, XOR7 = 0x141, XOR8 = 0x128;
__device__ __forceinline__ float x16_max(float x) {
  auto s = __builtin_amdgcn_permlane16_swap(__float_as_uint(x), __float_as_uint(x), false, false);
  x = fmaxf(__uint_as_float(s[0]), __uint_as_float(s[1]));
  auto t = __builtin_amdgcn_permlane32_swap(__float_as_uint(x), __float_as_uint(x), false, false);
  return fmaxf(__uint_as_float(t[0]), __uint_as_float(t[1]));
}
__device__ __forceinline__ float x16_sum(float x) {
  auto s = __builtin_amdgcn_permlane16_swap(__float_as_uint(x), __float_as_uint(x), false, false);
  x = __uint_as_float(s[0]) + __uint_as_float(s[1]);
  auto t = __builtin_amdgcn_permlane32_swap(__float_as_uint(x), __float_as_uint(x), false, false);
  return __uint_as_float(t[0]) + __uint_as_float(t[1]);
}
__device__ __forceinline__ float wave_sum(float x) {
  x += dpp<XOR1>(x); x += dpp<XOR2>(x); x += dpp<XOR7>(x); x += dpp<XOR8>(x);
  return x16_sum(x);
}

__device__ __forceinline__ void block_layernorm(const unsigned short* __restrict__ x, float* xs, float* red, int tid) {
  const int lane = tid & 63;
  float v[10];
#pragma unroll
  for (int i = 0; i < 10; ++i) v[i] = bf2f(x[lane + 64 * i]);
  float s = 0.f;
#pragma unroll
  for (int i = 0; i < 10; ++i) s += v[i];
  s = wave_sum(s);
  const float mean = s * (1.f / DM);
  float q = 0.f;
#pragma unroll
  for (int i = 0; i < 10; ++i) { float d = v[i] - mean; q += d * d; }
  q = wave_sum(q);
  const float var = q * (1.f / DM);
  const float rs = 1.f / sqrtf(var + 1e-5f);
  if (tid < 64) {
#pragma unroll
    for (int i = 0; i < 10; ++i) xs[lane + 64 * i] = (v[i] - mean) * rs;
  }
}

__global__ void __launch_bounds__(256) k_prep(const unsigned short* __restrict__ x, const unsigned short* __restrict__ wq,
                                              const unsigned short* __restrict__ w1, const unsigned short* __restrict__ b1,
                                              unsigned char* __restrict__ ws) {
  __shared__ float xs[DM];
  __shared__ float red[4][32];
  const int tid = threadIdx.x, lane = tid & 63, wave = tid >> 6;
  const int j = blockIdx.x;
  const bool isq = j < 192;
  const unsigned short* W = isq ? wq : w1;
  const int ld = isq ? HD : FF;
  const int col0 = (isq ? j : j - 192) * 32;
  const int c = tid & 3, r0 = tid >> 2;
  u32x4 w[10];
#pragma unroll
  for (int i = 0; i < 10; ++i)
    w[i] = __builtin_nontemporal_load(reinterpret_cast<const u32x4*>(W + (size_t)(r0 + 64 * i) * ld + col0 + 8 * c));
  block_layernorm(x, xs, nullptr, tid);
  __syncthreads();
  float acc[8];
#pragma unroll
  for (int k = 0; k < 8; ++k) acc[k] = 0.f;
#pragma unroll
  for (int i = 0; i < 10; ++i) {
    const float xv = xs[r0 + 64 * i];
#pragma unroll
    for (int k = 0; k < 4; ++k) { acc[2 * k] = fmaf(xv, bflo(w[i][k]), acc[2 * k]); acc[2 * k + 1] = fmaf(xv, bfhi(w[i][k]), acc[2 * k + 1]); }
  }
#pragma unroll
  for (int k = 0; k < 8; ++k) {
    float a = acc[k];
    a += __shfl_xor(a, 4); a += dpp<XOR8>(a); a = x16_sum(a);
    acc[k] = a;
  }
  if (lane < 4) {
#pragma unroll
    for (int k = 0; k < 8; ++k) red[wave][lane * 8 + k] = acc[k];
  }
  __syncthreads();
  if (tid < 32) {
    float a = (red[0][tid] + red[1][tid]) + (red[2][tid] + red[3][tid]);
    if (isq) {
      reinterpret_cast<unsigned short*>(ws + WS_Q)[col0 + tid] = f2bf(a);
    } else {
      float hv = a + bf2f(b1[col0 + tid]);
      const float u = 0.7978845608028654f * (hv + 0.044715f * hv * hv * hv);
      float g = 0.5f * hv * (1.f + tanhf(u));
      reinterpret_cast<float*>(ws + WS_H)[col0 + tid] = g;
    }
  }
}

__global__ void __launch_bounds__(NWAVES * 64) k_attn(const unsigned short* __restrict__ Kc, const unsigned short* __restrict__ Vc,
                                                      unsigned char* __restrict__ ws) {
  __shared__ float mrg[NWAVES][PREC];
  const int tid = threadIdx.x, lane = tid & 63, wave = __builtin_amdgcn_readfirstlane(tid >> 6);
  const int r = lane >> 3, sub = lane & 7;
  const int bid = blockIdx.x;
  const int h = bid / NSPLIT, sp = bid % NSPLIT;
  const unsigned* qp = reinterpret_cast<const unsigned*>(ws + WS_Q) + h * (DH / 2);
  u32x4 q[3];
#pragma unroll
  for (int j = 0; j < 3; ++j) q[j] = *reinterpret_cast<const u32x4*>(qp + j * 32 + sub * 4);
  const size_t base = ((size_t)h * SK + (size_t)sp * KPW + (size_t)wave * TILE_ROWS + r) * (DH * 2) + sub * 16;
  const unsigned char* kp = reinterpret_cast<const unsigned char*>(Kc) + base;
  const unsigned char* vp = reinterpret_cast<const unsigned char*>(Vc) + base;
  constexpr size_t STEP = (size_t)NWAVES * TILE_ROWS * DH * 2;
  constexpr float SC = 0.07216878364870322f;
  constexpr float LOG2E = 1.4426950408889634f;
  float m = -INFINITY, l = 0.f, acc[24];
#pragma unroll
  for (int e = 0; e < 24; ++e) acc[e] = 0.f;
  for (int it = 0; it < NTILES; ++it) {
    u32x4 kr[TSTEP][3], vr[TSTEP][3];
#pragma unroll
    for (int t = 0; t < TSTEP; ++t)
#pragma unroll
      for (int j = 0; j < 3; ++j) kr[t][j] = __builtin_nontemporal_load(reinterpret_cast<const u32x4*>(kp + t * 3072 + j * 128));
#pragma unroll
    for (int t = 0; t < TSTEP; ++t)
#pragma unroll
      for (int j = 0; j < 3; ++j) vr[t][j] = __builtin_nontemporal_load(reinterpret_cast<const u32x4*>(vp + t * 3072 + j * 128));
    kp += STEP; vp += STEP;
    float s[TSTEP];
#pragma unroll
    for (int t = 0; t < TSTEP; ++t) {
      float a0 = 0.f, a1 = 0.f, a2 = 0.f;
#pragma unroll
      for (int k = 0; k < 4; ++k) { a0 = dot2(kr[t][0][k], q[0][k], a0); a1 = dot2(kr[t][1][k], q[1][k], a1); a2 = dot2(kr[t][2][k], q[2][k], a2); }
      float a = (a0 + a1) + a2;
      a += dpp<XOR1>(a); a += dpp<XOR2>(a); a += dpp<XOR7>(a);
      s[t] = a * (SC * LOG2E);
    }
    float mx = s[0];
#pragma unroll
    for (int t = 1; t < TSTEP; ++t) mx = fmaxf(mx, s[t]);
    if (__any(mx > m)) {
      const float mn = fmaxf(m, mx);
      const float al = __builtin_amdgcn_exp2f(m - mn);
      l *= al;
#pragma unroll
      for (int e = 0; e < 24; ++e) acc[e] *= al;
      m = mn;
    }
#pragma unroll
    for (int t = 0; t < TSTEP; ++t) {
      const float p = __builtin_amdgcn_exp2f(s[t] - m);
      l += p;
#pragma unroll
      for (int j = 0; j < 3; ++j)
#pragma unroll
        for (int k = 0; k < 4; ++k) {
          acc[j * 8 + 2 * k] = fmaf(p, bflo(vr[t][j][k]), acc[j * 8 + 2 * k]);
          acc[j * 8 + 2 * k + 1] = fmaf(p, bfhi(vr[t][j][k]), acc[j * 8 + 2 * k + 1]);
        }
    }
  }
  float M = fmaxf(m, dpp<XOR8>(m)); M = x16_max(M);
  const float f = __builtin_amdgcn_exp2f(m - M);
  l *= f; l += dpp<XOR8>(l); l = x16_sum(l);
#pragma unroll
  for (int e = 0; e < 24; ++e) { float a = acc[e] * f; a += dpp<XOR8>(a); acc[e] = x16_sum(a); }
  if (lane < 8) {
#pragma unroll
    for (int j = 0; j < 3; ++j) {
      *reinterpret_cast<f32x4*>(&mrg[wave][j * 64 + sub * 8]) = f32x4{acc[j * 8], acc[j * 8 + 1], acc[j * 8 + 2], acc[j * 8 + 3]};
      *reinterpret_cast<f32x4*>(&mrg[wave][j * 64 + sub * 8 + 4]) = f32x4{acc[j * 8 + 4], acc[j * 8 + 5], acc[j * 8 + 6], acc[j * 8 + 7]};
    }
    if (lane == 0) { mrg[wave][192] = M; mrg[wave][193] = l; }
  }
  __syncthreads();
  if (tid < DH) {
    float MM = mrg[0][192];
#pragma unroll
    for (int w = 1; w < NWAVES; ++w) MM = fmaxf(MM, mrg[w][192]);
    float L = 0.f, o = 0.f;
#pragma unroll
    for (int w = 0; w < NWAVES; ++w) { const float wt = __builtin_amdgcn_exp2f(mrg[w][192] - MM); L = fmaf(wt, mrg[w][193], L); o = fmaf(wt, mrg[w][tid], o); }
    float* rec = reinterpret_cast<float*>(ws + WS_PART) + (size_t)bid * PREC;
    rec[tid] = o;
    if (tid == 0) { rec[192] = MM; rec[193] = L; }
  }
}

__global__ void __launch_bounds__(256) k_proj(const unsigned short* __restrict__ wo, const unsigned short* __restrict__ w2,
                                              unsigned char* __restrict__ ws) {
  __shared__ float ov[24];
  __shared__ float red[3][DM];
  const int tid = threadIdx.x, j = blockIdx.x;
  const bool iso = j < 256;
  const unsigned short* W = iso ? wo + (size_t)j * 24 * DM : w2 + (size_t)(j - 256) * 24 * DM;
  const int cch = tid % 80, rg = tid / 80;
  u32x4 w[8];
  if (tid < 240) {
#pragma unroll
    for (int i = 0; i < 8; ++i) w[i] = __builtin_nontemporal_load(reinterpret_cast<const u32x4*>(W + (size_t)(rg * 8 + i) * DM + cch * 8));
  }
  if (tid < 24) {
    if (iso) {
      const int h = j >> 3, d = (j & 7) * 24 + tid;
      const float* rec = reinterpret_cast<const float*>(ws + WS_PART) + (size_t)h * NSPLIT * PREC;
      float MM = -INFINITY;
      for (int s = 0; s < NSPLIT; ++s) MM = fmaxf(MM, rec[s * PREC + 192]);
      float L = 0.f, o = 0.f;
      for (int s = 0; s < NSPLIT; ++s) { const float wt = __builtin_amdgcn_exp2f(rec[s * PREC + 192] - MM); L = fmaf(wt, rec[s * PREC + 193], L); o = fmaf(wt, rec[s * PREC + d], o); }
      o = o / L;
      ov[tid] = o;
    } else {
      ov[tid] = reinterpret_cast<const float*>(ws + WS_H)[(j - 256) * 24 + tid];
    }
  }
  __syncthreads();
  if (tid < 240) {
    float acc[8];
#pragma unroll
    for (int k = 0; k < 8; ++k) acc[k] = 0.f;
#pragma unroll
    for (int i = 0; i < 8; ++i) {
      const float xv = ov[rg * 8 + i];
#pragma unroll
      for (int k = 0; k < 4; ++k) { acc[2 * k] = fmaf(xv, bflo(w[i][k]), acc[2 * k]); acc[2 * k + 1] = fmaf(xv, bfhi(w[i][k]), acc[2 * k + 1]); }
    }
#pragma unroll
    for (int k = 0; k < 8; ++k) red[rg][cch * 8 + k] = acc[k];
  }
  __syncthreads();
  float* dst = iso ? reinterpret_cast<float*>(ws + WS_Y) + (size_t)j * DM : reinterpret_cast<float*>(ws + WS_Z) + (size_t)(j - 256) * DM;
  for (int c = tid; c < DM; c += 256) dst[c] = red[0][c] + red[1][c] + red[2][c];
}

__global__ void __launch_bounds__(256) k_final(const unsigned short* __restrict__ x, const unsigned short* __restrict__ b2,
                                               const unsigned char* __restrict__ ws, unsigned short* __restrict__ out) {
  __shared__ float ry[8][32], rz[8][32];
  const int tid = threadIdx.x, c = tid & 31, g = tid >> 5;
  const int col = blockIdx.x * 32 + c;
  const float* Y = reinterpret_cast<const float*>(ws + WS_Y);
  const float* Z = reinterpret_cast<const float*>(ws + WS_Z);
  float sy = 0.f, sz = 0.f;
#pragma unroll
  for (int i = 0; i < 32; ++i) sy += Y[(size_t)(g + 8 * i) * DM + col];
#pragma unroll
  for (int i = 0; i < 8; ++i) sz += Z[(size_t)(g + 8 * i) * DM + col];
  ry[g][c] = sy; rz[g][c] = sz;
  __syncthreads();
  if (tid < 32) {
    float s = 0.f, z = 0.f;
#pragma unroll
    for (int i = 0; i < 8; ++i) { s += ry[i][c]; z += rz[i][c]; }
    z = z + bf2f(b2[col]);
    float gte = z / (1.f + __expf(-z));
    float o = bf2f(x[col]) + s * gte;
    out[col] = f2bf(o);
  }
}

extern "C" void kernel_launch(void* const* d_in, const int* in_sizes, int n_in,
                              void* d_out, int out_size, void* d_ws, size_t ws_size,
                              hipStream_t stream) {
  const unsigned short* x = (const unsigned short*)d_in[0];
  const unsigned short* kc = (const unsigned short*)d_in[1];
  const unsigned short* vc = (const unsigned short*)d_in[2];
  const unsigned short* wq = (const unsigned short*)d_in[3];
  const unsigned short* wo = (const unsigned short*)d_in[4];
  const unsigned short* w1 = (const unsigned short*)d_in[6];
  const unsigned short* b1 = (const unsigned short*)d_in[7];
  const unsigned short* w2 = (const unsigned short*)d_in[8];
  const unsigned short* b2 = (const unsigned short*)d_in[9];
  unsigned char* ws = (unsigned char*)d_ws;
  k_prep<<<240, 256, 0, stream>>>(x, wq, w1, b1, ws);
  k_attn<<<NH * NSPLIT, NWAVES * 64, 0, stream>>>(kc, vc, ws);
  k_proj<<<256 + 64, 256, 0, stream>>>(wo, w2, ws);
  k_final<<<20, 256, 0, stream>>>(x, b2, ws, (unsigned short*)d_out);
}
```

```cpp
#include <hip/hip_runtime.h>
#include <hip/hip_bf16.h>
#include <stdint.h>

constexpr int DM = 640, NH = 32, SK = 32768, DH = 192, FF = 1536, HD = NH * DH;
constexpr int NW = 8;
#define TS 2
#define SPT 208
#define CHT 8
#define PFD 12
#define PACE0 200
#define PACED 44
constexpr int NT = NW * 64;
constexpr int NSPL = 8, NWG = NH * NSPL;
constexpr int TR = 8 * TS, NTILE = SK / TR;
constexpr int DYN0 = NSPL * SPT, NCH = (NTILE - DYN0) / CHT;
static_assert((NTILE - DYN0) % CHT == 0 && PFD <= SPT - 16 && PFD >= NW, "");
constexpr int PREC = 200;
constexpr int NOWN = 80;

constexpr int WS_EPOCH = 0;
constexpr int WS_CNT = 4096;
constexpr int WS_QX = 16384;
constexpr int WS_PX = WS_QX + NWG * 2048;
constexpr int WS_YX = WS_PX + NWG * 2048;
constexpr int WS_ZX = WS_YX + NOWN * NWG * 64;
constexpr int WS_END = WS_ZX + NOWN * NWG * 64;

typedef unsigned u32x4 __attribute__((ext_vector_type(4)));
typedef unsigned u32x2 __attribute__((ext_vector_type(2)));
typedef float f32x4 __attribute__((ext_vector_type(4)));
typedef float f32x2 __attribute__((ext_vector_type(2)));
typedef __bf16 bf16x2 __attribute__((ext_vector_type(2)));

__device__ __forceinline__ float bf2f(unsigned short u) { return __builtin_bit_cast(float, (unsigned)u << 16); }
__device__ __forceinline__ float bflo(unsigned u) { return __builtin_bit_cast(float, u << 16); }
__device__ __forceinline__ float bfhi(unsigned u) { return __builtin_bit_cast(float, u & 0xffff0000u); }
__device__ __forceinline__ float rbf(float x) { return (float)(__bf16)x; }
__device__ __forceinline__ unsigned short f2bf(float x) { return __builtin_bit_cast(unsigned short, (__bf16)x); }
__device__ __forceinline__ float dot2(unsigned k, unsigned q, float acc) {
  return __builtin_amdgcn_fdot2_f32_bf16(__builtin_bit_cast(bf16x2, k), __builtin_bit_cast(bf16x2, q), acc, false);
}
template <int CTRL> __device__ __forceinline__ float dpp(float x) {
  return __builtin_bit_cast(float, __builtin_amdgcn_mov_dpp(__builtin_bit_cast(int, x), CTRL, 0xf, 0xf, true));
}
constexpr int XOR1 = 0xB1, XOR2 = 0x4E, XOR7 = 0x141, XOR8 = 0x128, ROR4 = 0x124;
__device__ __forceinline__ float x16_max(float x) {
  auto s = __builtin_amdgcn_permlane16_swap(__float_as_uint(x), __float_as_uint(x), false, false);
  x = fmaxf(__uint_as_float(s[0]), __uint_as_float(s[1]));
  auto t = __builtin_amdgcn_permlane32_swap(__float_as_uint(x), __float_as_uint(x), false, false);
  return fmaxf(__uint_as_float(t[0]), __uint_as_float(t[1]));
}
__device__ __forceinline__ float x16_sum(float x) {
  auto s = __builtin_amdgcn_permlane16_swap(__float_as_uint(x), __float_as_uint(x), false, false);
  x = __uint_as_float(s[0]) + __uint_as_float(s[1]);
  auto t = __builtin_amdgcn_permlane32_swap(__float_as_uint(x), __float_as_uint(x), false, false);
  return __uint_as_float(t[0]) + __uint_as_float(t[1]);
}
__device__ __forceinline__ float wave_sum(float x) {
  x += dpp<XOR1>(x); x += dpp<XOR2>(x); x += dpp<XOR7>(x); x += dpp<XOR8>(x);
  return x16_sum(x);
}
__device__ __forceinline__ float rg_sum(float x) { x += dpp<XOR8>(x); return x16_sum(x); }
__device__ __forceinline__ float rdlane(float x, int l) { return __builtin_bit_cast(float, __builtin_amdgcn_readlane(__builtin_bit_cast(int, x), l)); }
#define LDNT(p) __builtin_nontemporal_load(reinterpret_cast<const u32x4*>(p))
#define WG_RLX __ATOMIC_RELAXED, __HIP_MEMORY_SCOPE_WORKGROUP
#define AG_RLX __ATOMIC_RELAXED, __HIP_MEMORY_SCOPE_AGENT
#define LLD(p) __builtin_amdgcn_readfirstlane(__hip_atomic_load((p), WG_RLX))
#define LDS_WAIT() asm volatile("s_waitcnt lgkmcnt(0)" ::: "memory")
constexpr int AUX_SC1 = 16;
constexpr int SPIN_CAP = 1 << 20;

#define STAMP(i) do {} while (0)

__device__ __forceinline__ void issue(u32x4 (&kr)[TS][3], u32x4 (&vr)[TS][3], const unsigned char* kp, const unsigned char* vp) {
#pragma unroll
  for (int t = 0; t < TS; ++t)
#pragma unroll
    for (int j = 0; j < 3; ++j) kr[t][j] = LDNT(kp + t * 3072 + j * 128);
#pragma unroll
  for (int t = 0; t < TS; ++t)
#pragma unroll
    for (int j = 0; j < 3; ++j) vr[t][j] = LDNT(vp + t * 3072 + j * 128);
}
__device__ __forceinline__ void compute(const u32x4 (&kr)[TS][3], const u32x4 (&vr)[TS][3], const u32x4 (&q)[3], float& m, float& l, float (&acc)[24]) {
  constexpr float SC = 0.07216878364870322f, LOG2E = 1.4426950408889634f;
  float s[TS];
#pragma unroll
  for (int t = 0; t < TS; ++t) {
    float a0 = 0.f, a1 = 0.f, a2 = 0.f;
#pragma unroll
    for (int k = 0; k < 4; ++k) { a0 = dot2(kr[t][0][k], q[0][k], a0); a1 = dot2(kr[t][1][k], q[1][k], a1); a2 = dot2(kr[t][2][k], q[2][k], a2); }
    float a = (a0 + a1) + a2;
    a += dpp<XOR1>(a); a += dpp<XOR2>(a); a += dpp<XOR7>(a);
    s[t] = rbf(rbf(a) * SC) * LOG2E;
  }
  float mx = s[0];
#pragma unroll
  for (int t = 1; t < TS; ++t) mx = fmaxf(mx, s[t]);
  if (__any(mx > m)) {
    const float mn = fmaxf(m, mx);
    const float al = __builtin_amdgcn_exp2f(m - mn);
    l *= al;
#pragma unroll
    for (int e = 0; e < 24; ++e) acc[e] *= al;
    m = mn;
  }
#pragma unroll
  for (int t = 0; t < TS; ++t) {
    const float p = __builtin_amdgcn_exp2f(s[t] - m);
    l += p;
#pragma unroll
    for (int j = 0; j < 3; ++j)
#pragma unroll
      for (int k = 0; k < 4; ++k) {
        acc[j * 8 + 2 * k] = fmaf(p, bflo(vr[t][j][k]), acc[j * 8 + 2 * k]);
        acc[j * 8 + 2 * k + 1] = fmaf(p, bfhi(vr[t][j][k]), acc[j * 8 + 2 * k + 1]);
      }
  }
}

__device__ __forceinline__ void put_g(__amdgpu_buffer_rsrc_t rs, int off, float a, float b, u32x2 tag) {
  u32x4 v = {__float_as_uint(a), __float_as_uint(b), tag[0], tag[1]};
  __builtin_amdgcn_raw_buffer_store_b128(v, rs, off, 0, AUX_SC1);
}
__device__ __forceinline__ bool tag_ok(u32x4 v, u32x2 tag) { return v[2] == tag[0] && v[3] == tag[1]; }
template <int N> __device__ __forceinline__ void poll_n(__amdgpu_buffer_rsrc_t rs, int off, int stride, u32x2 tag, u32x4 (&v)[N]) {
#pragma unroll 1
  for (int spin = 0; spin < SPIN_CAP; ++spin) {
    asm volatile("" : "+v"(off) :: "memory");
    bool ok = true;
#pragma unroll
    for (int i = 0; i < N; ++i) v[i] = __builtin_amdgcn_raw_buffer_load_b128(rs, off + i * stride, 0, AUX_SC1);
#pragma unroll
    for (int i = 0; i < N; ++i) ok = ok && tag_ok(v[i], tag);
    if (ok) break;
    __builtin_amdgcn_s_sleep(1);
  }
}

constexpr int L_WO = 0;
constexpr int L_W2 = L_WO + 24 * DM * 2;
constexpr int L_MRG = L_W2 + 6 * DM * 2;
constexpr int L_Q = L_MRG + 10 * 240 * 4;
constexpr int L_OV = L_Q + 512;
constexpr int L_RED = L_OV + 128;
constexpr int L_CTL = L_RED + NW * 16 * 4;
constexpr int L_TAB = L_CTL + 64;
constexpr int L_END = L_TAB + (NCH + 64) * 4;
constexpr int L_BYTES = L_END > 84 * 1024 ? L_END : 84 * 1024;
static_assert(NW * PREC <= 10 * 240, "");

__global__ void __launch_bounds__(NT, NW / 4) k_main(const unsigned char* __restrict__ Kc, const unsigned char* __restrict__ Vc,
                                                     const unsigned short* __restrict__ wq, const unsigned short* __restrict__ wo,
                                                     const unsigned short* __restrict__ w1, const unsigned short* __restrict__ b1,
                                                     const unsigned short* __restrict__ w2, const unsigned short* __restrict__ b2,
                                                     const unsigned short* __restrict__ x, unsigned char* ws, unsigned short* __restrict__ out) {
  __shared__ __attribute__((aligned(16))) unsigned char lds[L_BYTES];
  float* mrg = reinterpret_cast<float*>(lds + L_MRG);
  unsigned* lq = reinterpret_cast<unsigned*>(lds + L_Q);
  float* ov = reinterpret_cast<float*>(lds + L_OV);
  float* red = reinterpret_cast<float*>(lds + L_RED);
  unsigned* ctl = reinterpret_cast<unsigned*>(lds + L_CTL);
  unsigned* table = reinterpret_cast<unsigned*>(lds + L_TAB);
  const int tid = threadIdx.x, lane = tid & 63, wave = __builtin_amdgcn_readfirstlane(tid >> 6);
  const int r = lane >> 3, sub = lane & 7;
  const int bid = blockIdx.x;
  const int h = bid >> 3, sp = bid & 7;
  const __amdgpu_buffer_rsrc_t rs = __builtin_amdgcn_make_buffer_rsrc((void*)ws, 0, WS_END, 0x00020000);
  const unsigned long long tstart = __builtin_amdgcn_s_memrealtime();
  STAMP(0);

  if (sp == 0 && tid == 0) __builtin_amdgcn_raw_buffer_store_b32(0u, rs, WS_CNT + h * 256, 0, AUX_SC1);
  const u32x2 ep = __builtin_amdgcn_raw_buffer_load_b64(rs, WS_EPOCH, 0, AUX_SC1);
  unsigned xa[5];
#pragma unroll
  for (int i = 0; i < 5; ++i) xa[i] = reinterpret_cast<const unsigned*>(x)[lane + 64 * i];
  const int row0 = sp * 80 + r;
  const bool two = wave < 2;
  const bool w1b = wave >= 2 && wave < 6;
  const int ga = wave, gb = two ? wave + 8 : wave - 2, gc = wave + 4;
  const unsigned short xra = x[row0 + 8 * ga], xrb = x[row0 + 8 * gb];
  unsigned short xrc = 0;
  if (w1b) xrc = x[row0 + 8 * gc];
  u32x4 wa[3], wb[3];
  {
    const unsigned char* p = reinterpret_cast<const unsigned char*>(wq) + ((size_t)(row0 + 8 * ga) * HD + h * DH) * 2 + sub * 16;
#pragma unroll
    for (int j = 0; j < 3; ++j) wa[j] = LDNT(p + j * 128);
  }
  if (two) {
    const unsigned char* p = reinterpret_cast<const unsigned char*>(wq) + ((size_t)(row0 + 8 * gb) * HD + h * DH) * 2 + sub * 16;
#pragma unroll
    for (int j = 0; j < 3; ++j) wb[j] = LDNT(p + j * 128);
  } else {
    const int sc = sub < 6 ? sub : 0;
    wb[0] = LDNT(reinterpret_cast<const unsigned char*>(w1) + ((size_t)(row0 + 8 * gb) * FF + h * 48) * 2 + sc * 16);
    wb[1] = wb[0];
    if (w1b) wb[1] = LDNT(reinterpret_cast<const unsigned char*>(w1) + ((size_t)(row0 + 8 * gc) * FF + h * 48) * 2 + sc * 16);
  }
  unsigned b1v = 0;
  if (tid >= 96 && tid < 99) b1v = reinterpret_cast<const unsigned*>(b1)[bid * 3 + (tid - 96)];
  u32x4 st[7];
  const int t6 = tid - 128;
  if (!two) {
    const unsigned char* wob = reinterpret_cast<const unsigned char*>(wo) + (size_t)bid * (24 * DM * 2);
    const unsigned char* w2b = reinterpret_cast<const unsigned char*>(w2) + (size_t)bid * (6 * DM * 2);
#pragma unroll
    for (int i = 0; i < 5; ++i) st[i] = LDNT(wob + (t6 + i * 384) * 16);
    st[5] = LDNT(w2b + t6 * 16);
    if (t6 < 96) st[6] = LDNT(w2b + (t6 + 384) * 16);
  }
  if (tid < 8) ctl[tid] = tid == 0 ? 12u : 0u;
  for (int i = tid; i < NCH + 64; i += NT) table[i] = 0;
  __syncthreads();

  float mb, den;
  {
    float s = 0.f;
#pragma unroll
    for (int i = 0; i < 5; ++i) s += bflo(xa[i]) + bfhi(xa[i]);
    s = wave_sum(s);
    const float mean = s * (1.f / DM);
    float q2 = 0.f;
#pragma unroll
    for (int i = 0; i < 5; ++i) { const float d0 = bflo(xa[i]) - mean, d1 = bfhi(xa[i]) - mean; q2 += d0 * d0 + d1 * d1; }
    q2 = wave_sum(q2);
    mb = rbf(mean);
    den = rbf(sqrtf(rbf(rbf(q2 * (1.f / DM)) + 1e-5f)));
  }
  {
    const float xna = rbf(rbf(bf2f(xra) - mb) / den), xnb = rbf(rbf(bf2f(xrb) - mb) / den), xnc = rbf(rbf(bf2f(xrc) - mb) / den);
    float pa[24];
#pragma unroll
    for (int j = 0; j < 3; ++j)
#pragma unroll
      for (int k = 0; k < 4; ++k) { pa[j * 8 + 2 * k] = xna * bflo(wa[j][k]); pa[j * 8 + 2 * k + 1] = xna * bfhi(wa[j][k]); }
    if (two) {
#pragma unroll
      for (int j = 0; j < 3; ++j)
#pragma unroll
        for (int k = 0; k < 4; ++k) { pa[j * 8 + 2 * k] = fmaf(xnb, bflo(wb[j][k]), pa[j * 8 + 2 * k]); pa[j * 8 + 2 * k + 1] = fmaf(xnb, bfhi(wb[j][k]), pa[j * 8 + 2 * k + 1]); }
    }
#pragma unroll
    for (int e = 0; e < 24; ++e) pa[e] = rg_sum(pa[e]);
    float* rec = mrg + wave * 240;
    if (lane < 8) {
#pragma unroll
      for (int j = 0; j < 3; ++j) {
        *reinterpret_cast<f32x4*>(rec + j * 64 + sub * 8) = f32x4{pa[j * 8], pa[j * 8 + 1], pa[j * 8 + 2], pa[j * 8 + 3]};
        *reinterpret_cast<f32x4*>(rec + j * 64 + sub * 8 + 4) = f32x4{pa[j * 8 + 4], pa[j * 8 + 5], pa[j * 8 + 6], pa[j * 8 + 7]};
      }
    }
    float pb[8];
    if (!two) {
      const float xc = w1b ? xnc : 0.f;
#pragma unroll
      for (int k = 0; k < 4; ++k) { pb[2 * k] = fmaf(xc, bflo(wb[1][k]), xnb * bflo(wb[0][k])); pb[2 * k + 1] = fmaf(xc, bfhi(wb[1][k]), xnb * bfhi(wb[0][k])); }
#pragma unroll
      for (int e = 0; e < 8; ++e) pb[e] = rg_sum(pb[e]);
    } else {
#pragma unroll
      for (int e = 0; e < 8; ++e) pb[e] = 0.f;
    }
    if (lane < 6) {
      *reinterpret_cast<f32x4*>(rec + 192 + sub * 8) = f32x4{pb[0], pb[1], pb[2], pb[3]};
      *reinterpret_cast<f32x4*>(rec + 192 + sub * 8 + 4) = f32x4{pb[4], pb[5], pb[6], pb[7]};
    }
    LDS_WAIT();
    if (lane == 0) __hip_atomic_fetch_add(&ctl[4], 1u, WG_RLX);
  }
  u32x2 tag;
  { const unsigned long long e = ((unsigned long long)ep[1] << 32 | ep[0]) + 1ull; tag[0] = __builtin_amdgcn_readfirstlane((unsigned)e); tag[1] = __builtin_amdgcn_readfirstlane((unsigned)(e >> 32)); }
  STAMP(1);

  const size_t base = ((size_t)h * SK + r) * (DH * 2) + sub * 16;
  const unsigned char* kp = Kc + base;
  const unsigned char* vp = Vc + base;
  u32x4 ka[TS][3], va[TS][3], kb[TS][3], vb[TS][3];

  if (two) {
#pragma unroll 1
    for (int spin = 0; spin < SPIN_CAP; ++spin) { if (LLD(&ctl[4]) >= (unsigned)NW) break; __builtin_amdgcn_s_sleep(1); }
    asm volatile("" ::: "memory");
    if (tid < 120) {
      float s0 = 0.f, s1 = 0.f;
#pragma unroll
      for (int w = 0; w < NW; ++w) { const f32x2 v = *reinterpret_cast<const f32x2*>(mrg + w * 240 + 2 * tid); s0 += v[0]; s1 += v[1]; }
      asm volatile("s_waitcnt vmcnt(0)" ::: "memory");
      put_g(rs, WS_QX + bid * 2048 + tid * 16, s0, s1, tag);
    }
    STAMP(2);
    if (tid < 99) {
      const int gi = tid < 96 ? tid : 96 + sp * 3 + (tid - 96);
      u32x4 v[8];
      poll_n<8>(rs, WS_QX + h * 8 * 2048 + gi * 16, 2048, tag, v);
      float s0 = 0.f, s1 = 0.f;
#pragma unroll
      for (int i = 0; i < 8; ++i) { s0 += __uint_as_float(v[i][0]); s1 += __uint_as_float(v[i][1]); }
      if (tid < 96) {
        lq[tid] = (unsigned)f2bf(s0) | ((unsigned)f2bf(s1) << 16);
      } else {
        float g[2];
#pragma unroll
        for (int k = 0; k < 2; ++k) {
          const float hv = rbf(rbf(k ? s1 : s0) + (k ? bfhi(b1v) : bflo(b1v)));
          const float u = 0.7978845608028654f * (hv + 0.044715f * hv * hv * hv);
          g[k] = rbf(0.5f * hv * (1.f + tanhf(u)));
        }
        *reinterpret_cast<f32x2*>(lq + 96 + 2 * (tid - 96)) = f32x2{g[0], g[1]};
      }
    }
    LDS_WAIT();
    if (lane == 0) __hip_atomic_fetch_add(&ctl[5], 1u, WG_RLX);
  } else {
    const unsigned long long ta = tstart + PACE0 + PACED * (wave - 2), tb = ta + PACED * 6;
#pragma unroll 1
    for (int spin = 0; spin < SPIN_CAP; ++spin) { if (__builtin_amdgcn_s_memrealtime() >= ta || LLD(&ctl[5]) >= 2u) break; __builtin_amdgcn_s_sleep(1); }
    { const size_t off = (size_t)(sp * SPT + wave - 2) * (TR * DH * 2); issue(ka, va, kp + off, vp + off); }
#pragma unroll
    for (int i = 0; i < 5; ++i) *reinterpret_cast<u32x4*>(lds + L_WO + (t6 + i * 384) * 16) = st[i];
    *reinterpret_cast<u32x4*>(lds + L_W2 + t6 * 16) = st[5];
    if (t6 < 96) *reinterpret_cast<u32x4*>(lds + L_W2 + (t6 + 384) * 16) = st[6];
    LDS_WAIT();
    if (lane == 0) __hip_atomic_fetch_add(&ctl[6], 1u, WG_RLX);
#pragma unroll 1
    for (int spin = 0; spin < SPIN_CAP; ++spin) { if (__builtin_amdgcn_s_memrealtime() >= tb || LLD(&ctl[5]) >= 2u) break; __builtin_amdgcn_s_sleep(1); }
    { const size_t off = (size_t)(sp * SPT + 6 + wave - 2) * (TR * DH * 2); issue(kb, vb, kp + off, vp + off); }
  }
#pragma unroll 1
  for (int spin = 0; spin < SPIN_CAP; ++spin) { if (LLD(&ctl[5]) >= 2u) break; __builtin_amdgcn_s_sleep(1); }
  asm volatile("" ::: "memory");
  u32x4 q[3];
#pragma unroll
  for (int j = 0; j < 3; ++j) q[j] = *reinterpret_cast<const u32x4*>(lq + j * 32 + sub * 4);
  STAMP(3);

  unsigned* cnt = reinterpret_cast<unsigned*>(ws + WS_CNT) + h * 64;
  unsigned zoff = 0; asm volatile("" : "+v"(zoff));
  float m = -INFINITY, l = 0.f, acc[24];
#pragma unroll
  for (int e = 0; e < 24; ++e) acc[e] = 0.f;

  int cur;
  { unsigned nx = 0; if (lane == 0) nx = __hip_atomic_fetch_add(&ctl[0], 1u, WG_RLX); cur = __builtin_amdgcn_readfirstlane(nx); }
  auto fetch = [&]() -> bool {
    int tile = -1;
    if (cur < SPT) tile = sp * SPT + cur;
    else {
      const int j = (cur - SPT) / CHT;
#pragma unroll 1
      for (int spin = 0; spin < SPIN_CAP; ++spin) {
        const unsigned g = LLD(&table[j]);
        if (g != 0) { tile = DYN0 + (g - 1) * CHT + (cur - SPT) % CHT; break; }
        if (LLD(&ctl[1]) != 0) { asm volatile("" ::: "memory"); if (LLD(&ctl[2]) == 0) { asm volatile("" ::: "memory"); if (LLD(&ctl[3]) <= (unsigned)j) break; } }
        __builtin_amdgcn_s_sleep(2);
      }
      if (tile < 0) return false;
    }
    const int fut = cur + PFD - SPT;
    bool req = fut >= 0 && (fut % CHT) == 0;
    if (req) {
      unsigned stp = 0;
      if (lane == 0) {
        __hip_atomic_fetch_add(&ctl[2], 1u, WG_RLX);
        LDS_WAIT();
        stp = __hip_atomic_load(&ctl[1], WG_RLX);
        if (stp) __hip_atomic_fetch_add(&ctl[2], ~0u, WG_RLX);
      }
      req = __builtin_amdgcn_readfirstlane(stp) == 0;
    }
    unsigned gx = 0;
    if (req && lane == 0) gx = __hip_atomic_fetch_add(cnt + zoff, 1u, AG_RLX);
    const size_t off = (size_t)tile * (TR * DH * 2);
    issue(ka, va, kp + off, vp + off);
    unsigned nx = 0;
    if (lane == 0) nx = __hip_atomic_fetch_add(&ctl[0], 1u, WG_RLX);
    if (req && lane == 0) {
      if (gx < (unsigned)NCH) { const unsigned p = __hip_atomic_fetch_add(&ctl[3], 1u, WG_RLX); __hip_atomic_store(&table[p], gx + 1u, WG_RLX); }
      else __hip_atomic_store(&ctl[1], 1u, WG_RLX);
      LDS_WAIT();
      __hip_atomic_fetch_add(&ctl[2], ~0u, WG_RLX);
    }
    cur = __builtin_amdgcn_readfirstlane(nx);
    return true;
  };
  bool has;
  if (!two) {
    compute(ka, va, q, m, l, acc);
    has = fetch();
    compute(kb, vb, q, m, l, acc);
  } else {
    has = fetch();
#pragma unroll 1
    for (int spin = 0; spin < SPIN_CAP; ++spin) { if (LLD(&ctl[6]) >= 6u) break; __builtin_amdgcn_s_sleep(1); }
    asm volatile("" ::: "memory");
    const float* hv = reinterpret_cast<const float*>(lq + 96);
#pragma unroll
    for (int k = 0; k < 3; ++k) {
      const int t = tid + 128 * k;
      if (t < 320) {
        float z0 = 0.f, z1 = 0.f;
#pragma unroll
        for (int i = 0; i < 6; ++i) { const unsigned wv = *reinterpret_cast<const unsigned*>(lds + L_W2 + i * (DM * 2) + t * 4); z0 = fmaf(hv[i], bflo(wv), z0); z1 = fmaf(hv[i], bfhi(wv), z1); }
        put_g(rs, WS_ZX + (t >> 2) * (NWG * 64) + bid * 64 + (t & 3) * 16, z0, z1, tag);
      }
    }
  }
  while (has) {
    compute(ka, va, q, m, l, acc);
    has = fetch();
  }
  STAMP(4);

  const bool owner = (bid % 3 == 0) && (bid / 3 < NOWN);
  const int own = bid / 3;
  {
    float M = fmaxf(m, dpp<XOR8>(m)); M = x16_max(M);
    const float f = __builtin_amdgcn_exp2f(m - M);
    l = rg_sum(l * f);
#pragma unroll
    for (int e = 0; e < 24; ++e) acc[e] = rg_sum(acc[e] * f);
    if (lane < 8) {
      float* rec = mrg + wave * PREC;
#pragma unroll
      for (int j = 0; j < 3; ++j) {
        *reinterpret_cast<f32x4*>(rec + j * 64 + sub * 8) = f32x4{acc[j * 8], acc[j * 8 + 1], acc[j * 8 + 2], acc[j * 8 + 3]};
        *reinterpret_cast<f32x4*>(rec + j * 64 + sub * 8 + 4) = f32x4{acc[j * 8 + 4], acc[j * 8 + 5], acc[j * 8 + 6], acc[j * 8 + 7]};
      }
      if (lane == 0) { rec[192] = M; rec[193] = l; }
    }
  }
  float sz0 = 0.f, sz1 = 0.f;
  unsigned short xv = 0, bv = 0;
  if (owner) {
    if (tid < 8) { xv = x[own * 8 + tid]; bv = b2[own * 8 + tid]; }
    u32x4 v[2];
    poll_n<2>(rs, WS_ZX + own * (NWG * 64) + tid * 16, NT * 16, tag, v);
    sz0 = __uint_as_float(v[0][0]) + __uint_as_float(v[1][0]); sz1 = __uint_as_float(v[0][1]) + __uint_as_float(v[1][1]);
    sz0 += dpp<ROR4>(sz0); sz0 = rg_sum(sz0);
    sz1 += dpp<ROR4>(sz1); sz1 = rg_sum(sz1);
  }
  __syncthreads();
  if (tid < 97) {
    float MM = mrg[192];
#pragma unroll
    for (int w = 1; w < NW; ++w) MM = fmaxf(MM, mrg[w * PREC + 192]);
    float o0 = 0.f, o1 = 0.f;
#pragma unroll
    for (int w = 0; w < NW; ++w) {
      const float wt = __builtin_amdgcn_exp2f(mrg[w * PREC + 192] - MM);
      const f32x2 v = *reinterpret_cast<const f32x2*>(mrg + w * PREC + 2 * tid);
      o0 = fmaf(wt, v[0], o0); o1 = fmaf(wt, v[1], o1);
    }
    if (tid == 96) o0 = MM;
    put_g(rs, WS_PX + bid * 2048 + tid * 16, o0, o1, tag);
  }
  STAMP(5);
  if (wave == 2) {
    u32x4 v[8];
#pragma unroll
    for (int i = 0; i < 8; ++i) v[i] = u32x4{0u, 0u, 0u, 0u};
    if (lane < 13) poll_n<8>(rs, WS_PX + h * 8 * 2048 + (lane < 12 ? sp * 12 + lane : 96) * 16, 2048, tag, v);
    float ms[8], MM = -INFINITY;
#pragma unroll
    for (int i = 0; i < 8; ++i) { ms[i] = rdlane(__uint_as_float(v[i][0]), 12); MM = fmaxf(MM, ms[i]); }
    float L = 0.f, o0 = 0.f, o1 = 0.f;
#pragma unroll
    for (int i = 0; i < 8; ++i) {
      const float wt = __builtin_amdgcn_exp2f(ms[i] - MM);
      L = fmaf(wt, rdlane(__uint_as_float(v[i][1]), 12), L);
      o0 = fmaf(wt, __uint_as_float(v[i][0]), o0); o1 = fmaf(wt, __uint_as_float(v[i][1]), o1);
    }
    if (lane < 12) *reinterpret_cast<f32x2*>(ov + 2 * lane) = f32x2{rbf(o0 / L), rbf(o1 / L)};
  }
  __syncthreads();
  STAMP(6);
  if (tid < 320) {
    float y0 = 0.f, y1 = 0.f;
#pragma unroll
    for (int d = 0; d < 24; ++d) { const unsigned wv = *reinterpret_cast<const unsigned*>(lds + L_WO + d * (DM * 2) + tid * 4); const float o = ov[d]; y0 = fmaf(o, bflo(wv), y0); y1 = fmaf(o, bfhi(wv), y1); }
    put_g(rs, WS_YX + (tid >> 2) * (NWG * 64) + bid * 64 + (tid & 3) * 16, y0, y1, tag);
  }
  STAMP(7);
  if (!owner) return;
  float sy0, sy1;
  {
    u32x4 v[2];
    poll_n<2>(rs, WS_YX + own * (NWG * 64) + tid * 16, NT * 16, tag, v);
    sy0 = __uint_as_float(v[0][0]) + __uint_as_float(v[1][0]); sy1 = __uint_as_float(v[0][1]) + __uint_as_float(v[1][1]);
  }
  STAMP(8);
  sy0 += dpp<ROR4>(sy0); sy0 = rg_sum(sy0);
  sy1 += dpp<ROR4>(sy1); sy1 = rg_sum(sy1);
  if (lane < 4) { float* rr = red + wave * 16; rr[lane * 2] = sy0; rr[lane * 2 + 1] = sy1; rr[8 + lane * 2] = sz0; rr[8 + lane * 2 + 1] = sz1; }
  __syncthreads();
  if (tid < 8) {
    float s = 0.f, z = 0.f;
#pragma unroll
    for (int w = 0; w < NW; ++w) { s += red[w * 16 + tid]; z += red[w * 16 + 8 + tid]; }
    s = rbf(s);
    z = rbf(rbf(z) + bf2f(bv));
    const float gte = rbf(z / (1.f + __expf(-z)));
    out[own * 8 + tid] = f2bf(bf2f(xv) + rbf(s * gte));
  }
  STAMP(9);
  if (bid == 0 && tid == 0) {
    __builtin_amdgcn_raw_buffer_store_b64(tag, rs, WS_EPOCH, 0, AUX_SC1);
  }
}

extern "C" void kernel_launch(void* const* d_in, const int* in_sizes, int n_in,
                              void* d_out, int out_size, void* d_ws, size_t ws_size,
                              hipStream_t stream) {
  const unsigned short* x = (const unsigned short*)d_in[0];
  const unsigned char* kc = (const unsigned char*)d_in[1];
  const unsigned char* vc = (const unsigned char*)d_in[2];
  const unsigned short* wq = (const unsigned short*)d_in[3];
  const unsigned short* wo = (const unsigned short*)d_in[4];
  const unsigned short* w1 = (const unsigned short*)d_in[6];
  const unsigned short* b1 = (const unsigned short*)d_in[7];
  const unsigned short* w2 = (const unsigned short*)d_in[8];
  const unsigned short* b2 = (const unsigned short*)d_in[9];
  unsigned char* ws = (unsigned char*)d_ws;
  k_main<<<NWG, NT, 0, stream>>>(kc, vc, wq, wo, w1, b1, w2, b2, x, ws, (unsigned short*)d_out);
}
```

```cpp
#include <hip/hip_runtime.h>
#include <hip/hip_bf16.h>
#include <stdint.h>

constexpr int DM = 640, NH = 32, SK = 32768, DH = 192, FF = 1536, HD = NH * DH;
constexpr int NW = 8;
#define TS 1
#define SPT 416
#define PACE0 400
#define PACED 22
#define NPOLL 3
#define STAG 9
#define DBUF 0
#define DBLIM (SPT - 40)
constexpr int NT = NW * 64;
constexpr int NSPL = 8, NWG = NH * NSPL;
constexpr int TR = 8 * TS, NTILE = SK / TR;
constexpr int DYN0 = NSPL * SPT, POOL = NTILE - DYN0;
#define N8 16
#define N4 32
#define N2 0
#define N1 0
#define LOWW 10
#define LOWE 6
constexpr int G16 = (POOL - 8 * N8 - 4 * N4 - 2 * N2 - N1) / 16;
static_assert(G16 * 16 + 8 * N8 + 4 * N4 + 2 * N2 + N1 == POOL && G16 >= 0, "");
constexpr int G8 = G16 + N8, G4 = G8 + N4, G2 = G4 + N2, NCH = G2 + N1;
constexpr int QCAP = POOL + 64;
constexpr int PREC = 200;
constexpr int NOWN = 40;

constexpr int WS_EPOCH = 0;
constexpr int WS_CNT = 4096;
constexpr int WS_QX = 16384;
constexpr int WS_PX = WS_QX + NWG * 2048;
constexpr int WS_YX = WS_PX + NWG * 2048;
constexpr int WS_ZX = WS_YX + NOWN * NH * 128;
constexpr int WS_END = WS_ZX + NOWN * NH * 128;

typedef unsigned u32x4 __attribute__((ext_vector_type(4)));
typedef unsigned u32x2 __attribute__((ext_vector_type(2)));
typedef float f32x4 __attribute__((ext_vector_type(4)));
typedef float f32x2 __attribute__((ext_vector_type(2)));
typedef __bf16 bf16x2 __attribute__((ext_vector_type(2)));

__device__ __forceinline__ float bf2f(unsigned short u) { return __builtin_bit_cast(float, (unsigned)u << 16); }
__device__ __forceinline__ float bflo(unsigned u) { return __builtin_bit_cast(float, u << 16); }
__device__ __forceinline__ float bfhi(unsigned u) { return __builtin_bit_cast(float, u & 0xffff0000u); }
__device__ __forceinline__ float rbf(float x) { return (float)(__bf16)x; }
__device__ __forceinline__ unsigned short f2bf(float x) { return __builtin_bit_cast(unsigned short, (__bf16)x); }
__device__ __forceinline__ float dot2(unsigned k, unsigned q, float acc) {
  return __builtin_amdgcn_fdot2_f32_bf16(__builtin_bit_cast(bf16x2, k), __builtin_bit_cast(bf16x2, q), acc, false);
}
template <int CTRL> __device__ __forceinline__ float dpp(float x) {
  return __builtin_bit_cast(float, __builtin_amdgcn_mov_dpp(__builtin_bit_cast(int, x), CTRL, 0xf, 0xf, true));
}
constexpr int XOR1 = 0xB1, XOR2 = 0x4E, XOR7 = 0x141, XOR8 = 0x128, ROR4 = 0x124;
__device__ __forceinline__ float x16_max(float x) {
  auto s = __builtin_amdgcn_permlane16_swap(__float_as_uint(x), __float_as_uint(x), false, false);
  x = fmaxf(__uint_as_float(s[0]), __uint_as_float(s[1]));
  auto t = __builtin_amdgcn_permlane32_swap(__float_as_uint(x), __float_as_uint(x), false, false);
  return fmaxf(__uint_as_float(t[0]), __uint_as_float(t[1]));
}
__device__ __forceinline__ float x16_sum(float x) {
  auto s = __builtin_amdgcn_permlane16_swap(__float_as_uint(x), __float_as_uint(x), false, false);
  x = __uint_as_float(s[0]) + __uint_as_float(s[1]);
  auto t = __builtin_amdgcn_permlane32_swap(__float_as_uint(x), __float_as_uint(x), false, false);
  return __uint_as_float(t[0]) + __uint_as_float(t[1]);
}
__device__ __forceinline__ float wave_sum(float x) {
  x += dpp<XOR1>(x); x += dpp<XOR2>(x); x += dpp<XOR7>(x); x += dpp<XOR8>(x);
  return x16_sum(x);
}
__device__ __forceinline__ float rg_sum(float x) { x += dpp<XOR8>(x); return x16_sum(x); }
__device__ __forceinline__ float sw32_add(float a, float b) {
  auto s = __builtin_amdgcn_permlane32_swap(__float_as_uint(a), __float_as_uint(b), false, false);
  return __uint_as_float(s[0]) + __uint_as_float(s[1]);
}
__device__ __forceinline__ float sw16_add(float a, float b) {
  auto s = __builtin_amdgcn_permlane16_swap(__float_as_uint(a), __float_as_uint(b), false, false);
  return __uint_as_float(s[0]) + __uint_as_float(s[1]);
}
__device__ __forceinline__ float tr_sum8(const float* v, bool b8) {
  float u[4], w[2];
#pragma unroll
  for (int k = 0; k < 4; ++k) u[k] = sw32_add(v[k], v[k + 4]);
#pragma unroll
  for (int k = 0; k < 2; ++k) w[k] = sw16_add(u[k], u[k + 2]);
  const float keep = b8 ? w[1] : w[0], send = b8 ? w[0] : w[1];
  return keep + dpp<XOR8>(send);
}
__device__ __forceinline__ float rdlane(float x, int l) { return __builtin_bit_cast(float, __builtin_amdgcn_readlane(__builtin_bit_cast(int, x), l)); }
#define LDNT(p) __builtin_nontemporal_load(reinterpret_cast<const u32x4*>(p))
#define WG_RLX __ATOMIC_RELAXED, __HIP_MEMORY_SCOPE_WORKGROUP
#define AG_RLX __ATOMIC_RELAXED, __HIP_MEMORY_SCOPE_AGENT
#define LLD(p) __builtin_amdgcn_readfirstlane(__hip_atomic_load((p), WG_RLX))
#define LDS_WAIT() asm volatile("s_waitcnt lgkmcnt(0)" ::: "memory")
constexpr int AUX_SC1 = 16;
constexpr int SPIN_CAP = 1 << 20;

#define STAMP(i) do {} while (0)

__device__ __forceinline__ void issue(u32x4 (&kr)[TS][3], u32x4 (&vr)[TS][3], const unsigned char* kp, const unsigned char* vp) {
#pragma unroll
  for (int t = 0; t < TS; ++t)
#pragma unroll
    for (int j = 0; j < 3; ++j) kr[t][j] = LDNT(kp + t * 3072 + j * 128);
#pragma unroll
  for (int t = 0; t < TS; ++t)
#pragma unroll
    for (int j = 0; j < 3; ++j) vr[t][j] = LDNT(vp + t * 3072 + j * 128);
}
__device__ __forceinline__ void compute(const u32x4 (&kr)[TS][3], const u32x4 (&vr)[TS][3], const u32x4 (&q)[3], float& m, float& l, float (&acc)[24]) {
  constexpr float SC = 0.07216878364870322f, LOG2E = 1.4426950408889634f;
  float s[TS];
#pragma unroll
  for (int t = 0; t < TS; ++t) {
    float a0 = 0.f, a1 = 0.f, a2 = 0.f;
#pragma unroll
    for (int k = 0; k < 4; ++k) { a0 = dot2(kr[t][0][k], q[0][k], a0); a1 = dot2(kr[t][1][k], q[1][k], a1); a2 = dot2(kr[t][2][k], q[2][k], a2); }
    float a = (a0 + a1) + a2;
    a += dpp<XOR1>(a); a += dpp<XOR2>(a); a += dpp<XOR7>(a);
    s[t] = rbf(rbf(a) * SC) * LOG2E;
  }
  float mx = s[0];
#pragma unroll
  for (int t = 1; t < TS; ++t) mx = fmaxf(mx, s[t]);
  if (__any(mx > m)) {
    const float mn = fmaxf(m, mx);
    const float al = __builtin_amdgcn_exp2f(m - mn);
    l *= al;
#pragma unroll
    for (int e = 0; e < 24; ++e) acc[e] *= al;
    m = mn;
  }
#pragma unroll
  for (int t = 0; t < TS; ++t) {
    const float p = __builtin_amdgcn_exp2f(s[t] - m);
    l += p;
#pragma unroll
    for (int j = 0; j < 3; ++j)
#pragma unroll
      for (int k = 0; k < 4; ++k) {
        acc[j * 8 + 2 * k] = fmaf(p, bflo(vr[t][j][k]), acc[j * 8 + 2 * k]);
        acc[j * 8 + 2 * k + 1] = fmaf(p, bfhi(vr[t][j][k]), acc[j * 8 + 2 * k + 1]);
      }
  }
}

template <bool B> struct BoolC { static constexpr bool value = B; };
__device__ __forceinline__ void put_g(__amdgpu_buffer_rsrc_t rs, int off, float a, float b, u32x2 tag) {
  u32x4 v = {__float_as_uint(a), __float_as_uint(b), tag[0], tag[1]};
  __builtin_amdgcn_raw_buffer_store_b128(v, rs, off, 0, AUX_SC1);
}
__device__ __forceinline__ bool tag_ok(u32x4 v, u32x2 tag) { return v[2] == tag[0] && v[3] == tag[1]; }
template <int N> __device__ __forceinline__ void poll_n(__amdgpu_buffer_rsrc_t rs, int off, int stride, u32x2 tag, u32x4 (&v)[N]) {
#pragma unroll 1
  for (int spin = 0; spin < SPIN_CAP; ++spin) {
    asm volatile("" : "+v"(off) :: "memory");
    bool ok = true;
#pragma unroll
    for (int i = 0; i < N; ++i) v[i] = __builtin_amdgcn_raw_buffer_load_b128(rs, off + i * stride, 0, AUX_SC1);
#pragma unroll
    for (int i = 0; i < N; ++i) ok = ok && tag_ok(v[i], tag);
    if (ok) break;
    __builtin_amdgcn_s_sleep(1);
  }
}

constexpr int L_W2 = 0;
constexpr int L_MRG = L_W2 + 64 * 256;
constexpr int L_Q = L_MRG + NW * 256 * 4;
constexpr int L_OV = L_Q + 96 * 4 + 64 * 4;
constexpr int L_RED = L_OV + 192 * 4;
constexpr int L_G = L_RED + NW * 128 * 4;
constexpr int L_CTL = L_G + 64;
constexpr int L_TAB = L_CTL + 64;
constexpr int L_END = L_TAB + QCAP * 4;
constexpr int L_BYTES = L_END > 84 * 1024 ? L_END : 84 * 1024;

__global__ void __launch_bounds__(NT, NW / 4) k_main(const unsigned char* __restrict__ Kc, const unsigned char* __restrict__ Vc,
                                                     const unsigned short* __restrict__ wq, const unsigned short* __restrict__ wo,
                                                     const unsigned short* __restrict__ w1, const unsigned short* __restrict__ b1,
                                                     const unsigned short* __restrict__ w2, const unsigned short* __restrict__ b2,
                                                     const unsigned short* __restrict__ x, unsigned char* ws, unsigned short* __restrict__ out) {
  __shared__ __attribute__((aligned(16))) unsigned char lds[L_BYTES];
  float* mrg = reinterpret_cast<float*>(lds + L_MRG);
  unsigned* lq = reinterpret_cast<unsigned*>(lds + L_Q);
  float* ov = reinterpret_cast<float*>(lds + L_OV);
  float* red = reinterpret_cast<float*>(lds + L_RED);
  unsigned* ctl = reinterpret_cast<unsigned*>(lds + L_CTL);
  unsigned* table = reinterpret_cast<unsigned*>(lds + L_TAB);
  const int tid = threadIdx.x, lane = tid & 63, wave = __builtin_amdgcn_readfirstlane(tid >> 6);
  const int r = lane >> 3, sub = lane & 7;
  const bool b8 = (lane & 8) != 0;
  const int bid = blockIdx.x;
  const int h = bid >> 3, sp = bid & 7;
  const __amdgpu_buffer_rsrc_t rs = __builtin_amdgcn_make_buffer_rsrc((void*)ws, 0, WS_END, 0x00020000);
  const unsigned long long tstart = __builtin_amdgcn_s_memrealtime();
  STAMP(0);

  const size_t base = ((size_t)h * SK + r) * (DH * 2) + sub * 16;
  const unsigned char* kp = Kc + base;
  const unsigned char* vp = Vc + base;
  u32x4 ka[TS][3], va[TS][3], kb[TS][3], vb[TS][3], kc[TS][3], vc[TS][3], kd[TS][3], vd[TS][3];
  u32x2 tag;
  const bool ffn = h < 24, prj = sp < 5;
  const bool two = wave < 2;
  auto prologue = [&](auto EXC) {
    constexpr bool EX = decltype(EXC)::value;
    if (EX) { if (sp == 0 && tid == 0) __builtin_amdgcn_raw_buffer_store_b32(0u, rs, WS_CNT + h * 256, 0, AUX_SC1); }
    unsigned xa[5];
#pragma unroll
    for (int i = 0; i < 5; ++i) xa[i] = reinterpret_cast<const unsigned*>(x)[lane + 64 * i];
    const int row0 = sp * 80 + r;
    const bool w1b = wave < 6;
    const int ga = wave, gb = EX ? wave + 8 : wave - 2, gc = (!EX && w1b) ? wave + 4 : gb;
    u32x4 wa[3], wb[3];
    {
      const unsigned char* p = reinterpret_cast<const unsigned char*>(wq) + ((size_t)(row0 + 8 * ga) * HD + h * DH) * 2 + sub * 16;
#pragma unroll
      for (int j = 0; j < 3; ++j) wa[j] = LDNT(p + j * 128);
    }
    if (EX) {
      const unsigned char* p = reinterpret_cast<const unsigned char*>(wq) + ((size_t)(row0 + 8 * gb) * HD + h * DH) * 2 + sub * 16;
#pragma unroll
      for (int j = 0; j < 3; ++j) wb[j] = LDNT(p + j * 128);
    } else {
      const unsigned char* p1 = reinterpret_cast<const unsigned char*>(w1);
      wb[0] = LDNT(p1 + (ffn ? ((size_t)(row0 + 8 * gb) * FF + h * 64) * 2 + sub * 16 : 0));
      wb[1] = LDNT(p1 + (ffn ? ((size_t)(row0 + 8 * gc) * FF + h * 64) * 2 + sub * 16 : 0));
    }
    const u32x2 ep = __builtin_amdgcn_raw_buffer_load_b64(rs, WS_EPOCH, 0, AUX_SC1);
    unsigned b1v = 0;
    if (EX) b1v = reinterpret_cast<const unsigned*>(b1)[ffn ? h * 32 + (lane & 31) : 0];
    __builtin_amdgcn_sched_barrier(0);
    STAMP(10);
    if (EX) {
      if (tid < 16) ctl[tid] = tid == 0 ? 24u : (tid == 4 ? 16u : 0u);
      for (int i = tid; i < QCAP; i += 128) table[i] = 0;
    }
    float mb, den;
    {
      float s = 0.f;
#pragma unroll
      for (int i = 0; i < 5; ++i) s += bflo(xa[i]) + bfhi(xa[i]);
      s = wave_sum(s);
      const float mean = s * (1.f / DM);
      float q2 = 0.f;
#pragma unroll
      for (int i = 0; i < 5; ++i) { const float d0 = bflo(xa[i]) - mean, d1 = bfhi(xa[i]) - mean; q2 += d0 * d0 + d1 * d1; }
      q2 = wave_sum(q2);
      mb = rbf(mean);
      den = rbf(sqrtf(rbf(rbf(q2 * (1.f / DM)) + 1e-5f)));
    }
    auto xnorm = [&](int g) -> float {
      const int d0 = (sp * 80 + 8 * g) >> 1, rg = d0 >> 6;
      unsigned v = xa[0];
#pragma unroll
      for (int i = 1; i < 5; ++i) { v = rg == i ? xa[i] : v; asm volatile("" : "+v"(v)); }
      v = (unsigned)__builtin_amdgcn_ds_bpermute(((d0 & 63) + (r >> 1)) << 2, (int)v);
      const float xv = (r & 1) ? bfhi(v) : bflo(v);
      return rbf(rbf(xv - mb) / den);
    };
    u32x4 st[3];
    const int t6 = tid - 128;
    __builtin_amdgcn_sched_barrier(0);
    if (!EX) {
      const unsigned char* w2b = reinterpret_cast<const unsigned char*>(w2) + (size_t)h * (64 * DM * 2) + sp * 256;
#pragma unroll
      for (int k = 0; k < 3; ++k) {
        const int i = t6 + 384 * k;
        st[k] = LDNT((ffn && prj && i < 1024) ? w2b + (i >> 4) * (DM * 2) + (i & 15) * 16 : reinterpret_cast<const unsigned char*>(w2));
      }
      const size_t off = (size_t)(sp * SPT + wave - 2) * (TR * DH * 2);
      issue(ka, va, kp + off, vp + off);
    }
    __builtin_amdgcn_sched_barrier(0);
    STAMP(12);
    {
      const float xna = xnorm(ga), xnb = xnorm(gb);
      float pa[24];
#pragma unroll
      for (int j = 0; j < 3; ++j)
#pragma unroll
        for (int k = 0; k < 4; ++k) { pa[j * 8 + 2 * k] = xna * bflo(wa[j][k]); pa[j * 8 + 2 * k + 1] = xna * bfhi(wa[j][k]); }
      if (EX) {
#pragma unroll
        for (int j = 0; j < 3; ++j)
#pragma unroll
          for (int k = 0; k < 4; ++k) { pa[j * 8 + 2 * k] = fmaf(xnb, bflo(wb[j][k]), pa[j * 8 + 2 * k]); pa[j * 8 + 2 * k + 1] = fmaf(xnb, bfhi(wb[j][k]), pa[j * 8 + 2 * k + 1]); }
      }
      float* rec = mrg + wave * 256 + sub * 8 + r;
#pragma unroll
      for (int j = 0; j < 3; ++j) rec[j * 64] = tr_sum8(pa + j * 8, b8);
      float pz = 0.f;
      if (!EX) {
        float pb[8];
        const float xc = w1b ? xnorm(gc) : 0.f;
#pragma unroll
        for (int k = 0; k < 4; ++k) { pb[2 * k] = fmaf(xc, bflo(wb[1][k]), xnb * bflo(wb[0][k])); pb[2 * k + 1] = fmaf(xc, bfhi(wb[1][k]), xnb * bfhi(wb[0][k])); }
        pz = tr_sum8(pb, b8);
      }
      rec[192] = pz;
    }
    { const unsigned long long e = ((unsigned long long)ep[1] << 32 | ep[0]) + 1ull; tag[0] = __builtin_amdgcn_readfirstlane((unsigned)e); tag[1] = __builtin_amdgcn_readfirstlane((unsigned)(e >> 32)); }
    LDS_WAIT();
    __builtin_amdgcn_s_barrier();
    asm volatile("" ::: "memory");
    STAMP(1);
    if (EX) {
      {
        float s0 = 0.f, s1 = 0.f;
#pragma unroll
        for (int w = 0; w < NW; ++w) { const f32x2 v = *reinterpret_cast<const f32x2*>(mrg + w * 256 + 2 * tid); s0 += v[0]; s1 += v[1]; }
        asm volatile("s_waitcnt vmcnt(0)" ::: "memory");
        put_g(rs, WS_QX + bid * 2048 + tid * 16, s0, s1, tag);
      }
      STAMP(2);
      {
        u32x4 v[8];
        poll_n<8>(rs, WS_QX + h * 8 * 2048 + tid * 16, 2048, tag, v);
        float s0 = 0.f, s1 = 0.f;
#pragma unroll
        for (int i = 0; i < 8; ++i) { s0 += __uint_as_float(v[i][0]); s1 += __uint_as_float(v[i][1]); }
        if (tid < 96) lq[tid] = (unsigned)f2bf(s0) | ((unsigned)f2bf(s1) << 16);
        LDS_WAIT();
        if (lane == 0) __hip_atomic_fetch_add(&ctl[5], 1u, WG_RLX);
        if (wave == 1) {
          float g[2];
#pragma unroll
          for (int k = 0; k < 2; ++k) {
            const float hv = rbf(rbf(k ? s1 : s0) + (k ? bfhi(b1v) : bflo(b1v)));
            const float u = 0.7978845608028654f * (hv + 0.044715f * hv * hv * hv);
            g[k] = rbf(0.5f * hv * (1.f + tanhf(u)));
          }
          if (tid >= 96) *reinterpret_cast<f32x2*>(lq + 96 + 2 * (tid - 96)) = f32x2{g[0], g[1]};
          LDS_WAIT();
          if (lane == 0) __hip_atomic_fetch_add(&ctl[6], 1u, WG_RLX);
        }
      }
      STAMP(14);
    } else {
      *reinterpret_cast<u32x4*>(lds + L_W2 + t6 * 16) = st[0];
      *reinterpret_cast<u32x4*>(lds + L_W2 + (t6 + 384) * 16) = st[1];
      if (t6 < 256) *reinterpret_cast<u32x4*>(lds + L_W2 + (t6 + 768) * 16) = st[2];
      LDS_WAIT();
      if (lane == 0) __hip_atomic_fetch_add(&ctl[6], 1u, WG_RLX);
      const unsigned long long ta = tstart + PACE0 + PACED * (wave - 2);
      auto pace = [&](unsigned long long t) {
#pragma unroll 1
        for (int spin = 0; spin < SPIN_CAP; ++spin) { if (__builtin_amdgcn_s_memrealtime() >= t || LLD(&ctl[5]) >= 2u) break; __builtin_amdgcn_s_sleep(1); }
      };
      pace(ta);
      { const size_t off = (size_t)(sp * SPT + 6 + wave - 2) * (TR * DH * 2); issue(kb, vb, kp + off, vp + off); }
      pace(ta + PACED * 6);
      { const size_t off = (size_t)(sp * SPT + 12 + wave - 2) * (TR * DH * 2); issue(kc, vc, kp + off, vp + off); }
      pace(ta + PACED * 12);
      { const size_t off = (size_t)(sp * SPT + 18 + wave - 2) * (TR * DH * 2); issue(kd, vd, kp + off, vp + off); }
    }
  };
  if (two) prologue(BoolC<true>{}); else prologue(BoolC<false>{});
#pragma unroll 1
  for (int spin = 0; spin < SPIN_CAP; ++spin) { if (LLD(&ctl[5]) >= 2u) break; __builtin_amdgcn_s_sleep(1); }
  asm volatile("" ::: "memory");
  u32x4 q[3];
#pragma unroll
  for (int j = 0; j < 3; ++j) q[j] = *reinterpret_cast<const u32x4*>(lq + j * 32 + sub * 4);
  STAMP(3);

  unsigned* cnt = reinterpret_cast<unsigned*>(ws + WS_CNT) + h * 64;
  unsigned zoff = 0; asm volatile("" : "+v"(zoff));
  float m = -INFINITY, l = 0.f, acc[24];
#pragma unroll
  for (int e = 0; e < 24; ++e) acc[e] = 0.f;

  unsigned long long tlast = 0, tcomp = 0;
  int cur;
  { unsigned nx = 0; if (lane == 0) nx = __hip_atomic_fetch_add(&ctl[0], 1u, WG_RLX); cur = __builtin_amdgcn_readfirstlane(nx); }
  const unsigned lds0 = (unsigned)(size_t)(__attribute__((address_space(3))) unsigned char*)lds;
  auto deliver = [&](unsigned gx) {
    const int g = __builtin_amdgcn_readfirstlane(gx);
    if ((unsigned)g < (unsigned)NCH) {
      int sz, st;
      if (g < G16) { sz = 16; st = g * 16; }
      else if (g < G8) { sz = 8; st = G16 * 16 + (g - G16) * 8; }
      else if (g < G4) { sz = 4; st = G16 * 16 + N8 * 8 + (g - G8) * 4; }
      else if (g < G2) { sz = 2; st = G16 * 16 + N8 * 8 + N4 * 4 + (g - G4) * 2; }
      else { sz = 1; st = G16 * 16 + N8 * 8 + N4 * 4 + N2 * 2 + (g - G2); }
      unsigned p = 0;
      if (lane == 0) { p = __hip_atomic_fetch_add(&ctl[3], (unsigned)sz, WG_RLX); __hip_atomic_fetch_min(&ctl[4], (unsigned)sz, WG_RLX); }
      p = __builtin_amdgcn_readfirstlane(p);
      if (lane < sz) __hip_atomic_store(&table[p + lane], (unsigned)(DYN0 + st + lane + 1), WG_RLX);
    } else if (lane == 0) __hip_atomic_store(&ctl[1], 1u, WG_RLX);
    LDS_WAIT();
    if (lane == 0) __hip_atomic_fetch_add(&ctl[2], ~0u, WG_RLX);
  };
  auto claim = [&](unsigned seen) -> bool {
    unsigned s2 = 0;
    if (lane == 0) {
      const unsigned old = __hip_atomic_fetch_add(&ctl[2], 1u, WG_RLX);
      LDS_WAIT();
      s2 = __hip_atomic_load(&ctl[1], WG_RLX) | (old > seen ? 1u : 0u);
      if (s2) __hip_atomic_fetch_add(&ctl[2], ~0u, WG_RLX);
    }
    return __builtin_amdgcn_readfirstlane(s2) == 0;
  };
  auto fetch2 = [&](u32x4 (&kx)[TS][3], u32x4 (&vx)[TS][3]) -> bool {
    int tile = sp * SPT + cur;
    bool req = false;
    if (cur >= SPT - LOWW - NW) {
      const int idx = cur - SPT;
      unsigned stp, outs, nq, lsz;
      const unsigned qaddr = lds0 + L_TAB + (unsigned)(idx > 0 ? idx : 0) * 4u;
#pragma unroll 1
      for (int spin = 0;; ++spin) {
        u32x4 c4; unsigned l4, tq;
        asm volatile("ds_read_b128 %0, %3 offset:%5\n\tds_read_b32 %1, %3 offset:%6\n\tds_read_b32 %2, %4\n\ts_waitcnt lgkmcnt(0)"
                     : "=&v"(c4), "=&v"(l4), "=&v"(tq) : "v"(lds0), "v"(qaddr), "i"(L_CTL), "i"(L_CTL + 16) : "memory");
        stp = __builtin_amdgcn_readfirstlane(c4[1]); outs = __builtin_amdgcn_readfirstlane(c4[2]); nq = __builtin_amdgcn_readfirstlane(c4[3]);
        lsz = __builtin_amdgcn_readfirstlane(l4);
        if (idx < 0) break;
        const unsigned g = __builtin_amdgcn_readfirstlane(tq);
        if (g != 0) { tile = (int)g - 1; break; }
        if (spin >= SPIN_CAP) return false;
        if (outs == 0 && nq <= (unsigned)idx) {
          if (stp != 0) return false;
          if (claim(0u)) {
            unsigned gx = 0;
            if (lane == 0) gx = __hip_atomic_fetch_add(cnt + zoff, 1u, AG_RLX);
            deliver(gx);
          }
        } else __builtin_amdgcn_s_sleep(1);
      }
      const int level = (int)nq + (int)(outs * lsz) - (idx + 1);
      req = stp == 0 && level < (lsz <= 2u ? LOWE : LOWW);
      if (req) req = claim(outs);
    }
    unsigned gx = 0;
    if (req && lane == 0) gx = __hip_atomic_fetch_add(cnt + zoff, 1u, AG_RLX);
    const size_t off = (size_t)tile * (TR * DH * 2);
    issue(kx, vx, kp + off, vp + off);
    unsigned nx = 0;
    if (lane == 0) nx = __hip_atomic_fetch_add(&ctl[0], 1u, WG_RLX);
    if (req) deliver(gx);
    cur = __builtin_amdgcn_readfirstlane(nx);
    return true;
  };
  bool has;
  if (!two) {
    compute(ka, va, q, m, l, acc);
    has = fetch2(ka, va);
    compute(kb, vb, q, m, l, acc);
    compute(kc, vc, q, m, l, acc);
    compute(kd, vd, q, m, l, acc);
  } else {
    has = fetch2(ka, va);
    if (wave == 0 && ffn && prj) {
#pragma unroll 1
      for (int spin = 0; spin < SPIN_CAP; ++spin) { if (LLD(&ctl[6]) >= 7u) break; __builtin_amdgcn_s_sleep(1); }
      asm volatile("" ::: "memory");
      const float* hv = reinterpret_cast<const float*>(lq + 96);
      float z0 = 0.f, z1 = 0.f;
#pragma unroll 8
      for (int i = 0; i < 64; ++i) { const unsigned wv = *reinterpret_cast<const unsigned*>(lds + L_W2 + i * 256 + lane * 4); const float hh = hv[i]; z0 = fmaf(hh, bflo(wv), z0); z1 = fmaf(hh, bfhi(wv), z1); }
      put_g(rs, WS_ZX + (sp * 8 + (lane >> 3)) * (NH * 128) + h * 128 + (lane & 7) * 16, z0, z1, tag);
    }
  }
  while (has) {
    compute(ka, va, q, m, l, acc);
    has = fetch2(ka, va);
  }
  STAMP(4);

  const int own = h * 3 + (sp - 5);
  const bool owner = !prj && own < NOWN;
  int tk;
  {
    float M = fmaxf(m, dpp<XOR8>(m)); M = x16_max(M);
    const float f = __builtin_amdgcn_exp2f(m - M);
    const float L = rg_sum(l * f);
#pragma unroll
    for (int e = 0; e < 24; ++e) acc[e] *= f;
    float* rec = mrg + wave * PREC;
#pragma unroll
    for (int j = 0; j < 3; ++j) rec[j * 64 + lane] = tr_sum8(acc + j * 8, b8);
    if (lane == 0) *reinterpret_cast<f32x2*>(rec + 192) = f32x2{M, L};
    LDS_WAIT();
    unsigned t = 0;
    if (lane == 0) t = __hip_atomic_fetch_add(&ctl[7], 1u, WG_RLX);
    tk = __builtin_amdgcn_readfirstlane(t);
    asm volatile("" ::: "memory");
  }
  STAMP(11);
  if (tk == NW - 1) {
    f32x2 ml[NW];
    float p[NW][3];
#pragma unroll
    for (int w = 0; w < NW; ++w) {
      ml[w] = *reinterpret_cast<const f32x2*>(mrg + w * PREC + 192);
#pragma unroll
      for (int j = 0; j < 3; ++j) p[w][j] = mrg[w * PREC + j * 64 + lane];
    }
    float MM = ml[0][0];
#pragma unroll
    for (int w = 1; w < NW; ++w) MM = fmaxf(MM, ml[w][0]);
    float L = 0.f, o[3] = {0.f, 0.f, 0.f};
#pragma unroll
    for (int w = 0; w < NW; ++w) {
      const float wt = __builtin_amdgcn_exp2f(ml[w][0] - MM);
      L = fmaf(wt, ml[w][1], L);
#pragma unroll
      for (int j = 0; j < 3; ++j) o[j] = fmaf(wt, p[w][j], o[j]);
    }
    const int off = WS_PX + bid * 2048 + sub * 256 + r * 32;
    put_g(rs, off, o[0], o[1], tag);
    put_g(rs, off + 16, o[2], b8 ? L : MM, tag);
    if (lane == 0) __hip_atomic_store(&ctl[8], 1u, WG_RLX);
    STAMP(5);
  }
  __builtin_amdgcn_sched_barrier(0);
  if (prj) {
    u32x4 wreg[6];
    {
      const unsigned char* wp = reinterpret_cast<const unsigned char*>(wo) + (size_t)(h * DH + wave * 4 + (lane >> 4)) * (DM * 2) + sp * 256 + (lane & 15) * 16;
#pragma unroll
      for (int k = 0; k < 6; ++k) wreg[k] = LDNT(wp + (size_t)k * 32 * (DM * 2));
    }
    if (tk < 2) {
#pragma unroll 1
      for (int spin = 0; spin < SPIN_CAP; ++spin) { if (LLD(&ctl[8]) != 0) break; __builtin_amdgcn_s_sleep(1); }
      u32x4 v[8];
#pragma unroll
      for (int i = 0; i < 8; ++i) v[i] = u32x4{0u, 0u, ~tag[0], 0u};
      int off = WS_PX + h * 8 * 2048 + (tk * 64 + lane) * 16;
#pragma unroll 1
      for (int spin = 0; spin < SPIN_CAP; ++spin) {
        asm volatile("" : "+v"(off) :: "memory");
        bool need[8];
#pragma unroll
        for (int i = 0; i < 8; ++i) need[i] = __any(!tag_ok(v[i], tag));
        bool any = false;
#pragma unroll
        for (int i = 0; i < 8; ++i) { any = any || need[i]; if (need[i]) v[i] = __builtin_amdgcn_raw_buffer_load_b128(rs, off + i * 2048, 0, AUX_SC1); }
        if (!any) break;
      }
      STAMP(15);
      float ms[8], MM = -INFINITY;
#pragma unroll
      for (int i = 0; i < 8; ++i) { ms[i] = rdlane(__uint_as_float(v[i][1]), 1); MM = fmaxf(MM, ms[i]); }
      float L = 0.f, o0 = 0.f, o1 = 0.f;
#pragma unroll
      for (int i = 0; i < 8; ++i) {
        const float wt = __builtin_amdgcn_exp2f(ms[i] - MM);
        L = fmaf(wt, rdlane(__uint_as_float(v[i][1]), 3), L);
        o0 = fmaf(wt, __uint_as_float(v[i][0]), o0); o1 = fmaf(wt, __uint_as_float(v[i][1]), o1);
      }
      const float inv = 1.f / L;
      const int d = (tk * 64 + lane) >> 1;
      if (lane & 1) ov[128 + d] = rbf(o0 * inv);
      else { ov[d] = rbf(o0 * inv); ov[64 + d] = rbf(o1 * inv); }
      LDS_WAIT();
      if (lane == 0) __hip_atomic_fetch_add(&ctl[9], 1u, WG_RLX);
      STAMP(13);
    }
#pragma unroll 1
    for (int spin = 0; spin < SPIN_CAP; ++spin) { if (LLD(&ctl[9]) >= 2u) break; __builtin_amdgcn_s_sleep(1); }
    asm volatile("" ::: "memory");
    STAMP(6);
    float y[8];
#pragma unroll
    for (int e = 0; e < 8; ++e) y[e] = 0.f;
#pragma unroll
    for (int k = 0; k < 6; ++k) {
      const float o = ov[k * 32 + wave * 4 + (lane >> 4)];
#pragma unroll
      for (int e = 0; e < 4; ++e) { y[2 * e] = fmaf(o, bflo(wreg[k][e]), y[2 * e]); y[2 * e + 1] = fmaf(o, bfhi(wreg[k][e]), y[2 * e + 1]); }
    }
    float u[4];
#pragma unroll
    for (int e = 0; e < 4; ++e) u[e] = sw32_add(y[e], y[e + 4]);
    const float w0 = sw16_add(u[0], u[2]), w1v = sw16_add(u[1], u[3]);
    *reinterpret_cast<f32x2*>(red + wave * 128 + (lane & 15) * 8 + (lane >> 4) * 2) = f32x2{w0, w1v};
    LDS_WAIT();
    unsigned t = 0;
    if (lane == 0) t = __hip_atomic_fetch_add(&ctl[10], 1u, WG_RLX);
    const int tk2 = __builtin_amdgcn_readfirstlane(t);
    asm volatile("" ::: "memory");
    if (tk2 == NW - 1) {
      float y0 = 0.f, y1 = 0.f;
#pragma unroll
      for (int w = 0; w < NW; ++w) { const f32x2 v = *reinterpret_cast<const f32x2*>(red + w * 128 + lane * 2); y0 += v[0]; y1 += v[1]; }
      put_g(rs, WS_YX + (sp * 8 + (lane >> 3)) * (NH * 128) + h * 128 + (lane & 7) * 16, y0, y1, tag);
      STAMP(7);
    }
    return;
  }
  if (!owner || tk != 0) return;
  {
    const unsigned xv = reinterpret_cast<const unsigned*>(x)[own * 8 + (lane & 7)];
    const unsigned bv = reinterpret_cast<const unsigned*>(b2)[own * 8 + (lane & 7)];
    float g0, g1;
    {
      u32x4 v[3];
      poll_n<3>(rs, WS_ZX + own * (NH * 128) + lane * 16, 1024, tag, v);
      float z0 = (__uint_as_float(v[0][0]) + __uint_as_float(v[1][0])) + __uint_as_float(v[2][0]);
      float z1 = (__uint_as_float(v[0][1]) + __uint_as_float(v[1][1])) + __uint_as_float(v[2][1]);
      z0 = rg_sum(z0); z1 = rg_sum(z1);
      z0 = rbf(rbf(z0) + bflo(bv)); z1 = rbf(rbf(z1) + bfhi(bv));
      g0 = rbf(z0 / (1.f + __expf(-z0))); g1 = rbf(z1 / (1.f + __expf(-z1)));
    }
#pragma unroll 1
    for (int spin = 0; spin < SPIN_CAP; ++spin) { if (LLD(&ctl[8]) != 0) break; __builtin_amdgcn_s_sleep(1); }
    u32x4 v[4];
#pragma unroll
    for (int i = 0; i < 4; ++i) v[i] = u32x4{0u, 0u, ~tag[0], 0u};
    int off = WS_YX + own * (NH * 128) + lane * 16;
#pragma unroll 1
    for (int spin = 0; spin < SPIN_CAP; ++spin) {
      asm volatile("" : "+v"(off) :: "memory");
      bool any = false;
#pragma unroll
      for (int i = 0; i < 4; ++i) { const bool nd = !tag_ok(v[i], tag); any = any || nd; if (nd) v[i] = __builtin_amdgcn_raw_buffer_load_b128(rs, off + i * 1024, 0, AUX_SC1); }
      if (!any) break;
    }
    STAMP(8);
    float s0 = (__uint_as_float(v[0][0]) + __uint_as_float(v[1][0])) + (__uint_as_float(v[2][0]) + __uint_as_float(v[3][0]));
    float s1 = (__uint_as_float(v[0][1]) + __uint_as_float(v[1][1])) + (__uint_as_float(v[2][1]) + __uint_as_float(v[3][1]));
    s0 = rg_sum(s0); s1 = rg_sum(s1);
    if (lane < 8) {
      const float o0 = bflo(xv) + rbf(rbf(s0) * g0), o1 = bfhi(xv) + rbf(rbf(s1) * g1);
      reinterpret_cast<unsigned*>(out)[own * 8 + lane] = (unsigned)f2bf(o0) | ((unsigned)f2bf(o1) << 16);
    }
    STAMP(9);
    if (own == 0 && lane == 0) __builtin_amdgcn_raw_buffer_store_b64(tag, rs, WS_EPOCH, 0, AUX_SC1);
  }
}

extern "C" void kernel_launch(void* const* d_in, const int* in_sizes, int n_in,
                              void* d_out, int out_size, void* d_ws, size_t ws_size,
                              hipStream_t stream) {
  const unsigned short* x = (const unsigned short*)d_in[0];
  const unsigned char* kc = (const unsigned char*)d_in[1];
  const unsigned char* vc = (const unsigned char*)d_in[2];
  const unsigned short* wq = (const unsigned short*)d_in[3];
  const unsigned short* wo = (const unsigned short*)d_in[4];
  const unsigned short* w1 = (const unsigned short*)d_in[6];
  const unsigned short* b1 = (const unsigned short*)d_in[7];
  const unsigned short* w2 = (const unsigned short*)d_in[8];
  const unsigned short* b2 = (const unsigned short*)d_in[9];
  unsigned char* ws = (unsigned char*)d_ws;
  k_main<<<NWG, NT, 0, stream>>>(kc, vc, wq, wo, w1, b1, w2, b2, x, ws, (unsigned short*)d_out);
}
```

```cpp
#include <hip/hip_runtime.h>
#include <hip/hip_bf16.h>
#include <stdint.h>

constexpr int DM = 640, NH = 32, SK = 32768, DH = 192, FF = 1536, HD = NH * DH;
constexpr int NW = 8;
#define TS 1
#define SPT 344
#define PACE0 400
#define PACED 22
#define NPOLL 3
#define STAG 9
#define DBUF 0
#define DBLIM (SPT - 40)
#define EARLYB 2
#define NPRE 5
constexpr int NT = NW * 64;
constexpr int NSPL = 8, NWG = NH * NSPL;
constexpr int TR = 8 * TS, NTILE = SK / TR;
constexpr int DYN0 = NSPL * SPT, POOL = NTILE - DYN0;
#define N8 (16 + (SPT & 1))
#define N4 32
#define N2 0
#define N1 0
#define LOWW 10
#define LOW4 LOWW
#define LOW8 LOWW
#define LOWE 6
#define N32 18
constexpr int N16 = (POOL - 32 * N32 - 8 * N8 - 4 * N4 - 2 * N2 - N1) / 16;
static_assert(N32 * 32 + N16 * 16 + 8 * N8 + 4 * N4 + 2 * N2 + N1 == POOL && N16 >= 0, "");
constexpr int G32 = N32, G16 = G32 + N16, S16 = 32 * N32, S8 = S16 + 16 * N16;
constexpr int G8 = G16 + N8, G4 = G8 + N4, G2 = G4 + N2, NCH = G2 + N1;
#define NSUB 3
#define RP 0
static_assert(NSUB == 1 || POOL % (32 * NSUB) == 0, "");
constexpr int QCAP = POOL + 64;
constexpr int PREC = 200;
constexpr int NOWN = 40;

constexpr int WS_EPOCH = 0;
constexpr int WS_CNT = 4096;
constexpr int WS_QX = 16384;
constexpr int WS_PX = WS_QX + NWG * 2048;
constexpr int WS_YX = WS_PX + NWG * 2048;
constexpr int WS_ZX = WS_YX + NOWN * NH * 128;
constexpr int WS_END = WS_ZX + NOWN * NH * 128;

typedef unsigned u32x4 __attribute__((ext_vector_type(4)));
typedef unsigned u32x2 __attribute__((ext_vector_type(2)));
typedef float f32x4 __attribute__((ext_vector_type(4)));
typedef float f32x2 __attribute__((ext_vector_type(2)));
typedef __bf16 bf16x2 __attribute__((ext_vector_type(2)));

__device__ __forceinline__ float bf2f(unsigned short u) { return __builtin_bit_cast(float, (unsigned)u << 16); }
__device__ __forceinline__ float bflo(unsigned u) { return __builtin_bit_cast(float, u << 16); }
__device__ __forceinline__ float bfhi(unsigned u) { return __builtin_bit_cast(float, u & 0xffff0000u); }
__device__ __forceinline__ float rbf(float x) { return (float)(__bf16)x; }
__device__ __forceinline__ unsigned short f2bf(float x) { return __builtin_bit_cast(unsigned short, (__bf16)x); }
__device__ __forceinline__ float dot2(unsigned k, unsigned q, float acc) {
  return __builtin_amdgcn_fdot2_f32_bf16(__builtin_bit_cast(bf16x2, k), __builtin_bit_cast(bf16x2, q), acc, false);
}
template <int CTRL> __device__ __forceinline__ float dpp(float x) {
  return __builtin_bit_cast(float, __builtin_amdgcn_mov_dpp(__builtin_bit_cast(int, x), CTRL, 0xf, 0xf, true));
}
constexpr int XOR1 = 0xB1, XOR2 = 0x4E, XOR7 = 0x141, XOR8 = 0x128, ROR4 = 0x124;
__device__ __forceinline__ float x16_max(float x) {
  auto s = __builtin_amdgcn_permlane16_swap(__float_as_uint(x), __float_as_uint(x), false, false);
  x = fmaxf(__uint_as_float(s[0]), __uint_as_float(s[1]));
  auto t = __builtin_amdgcn_permlane32_swap(__float_as_uint(x), __float_as_uint(x), false, false);
  return fmaxf(__uint_as_float(t[0]), __uint_as_float(t[1]));
}
__device__ __forceinline__ float x16_sum(float x) {
  auto s = __builtin_amdgcn_permlane16_swap(__float_as_uint(x), __float_as_uint(x), false, false);
  x = __uint_as_float(s[0]) + __uint_as_float(s[1]);
  auto t = __builtin_amdgcn_permlane32_swap(__float_as_uint(x), __float_as_uint(x), false, false);
  return __uint_as_float(t[0]) + __uint_as_float(t[1]);
}
__device__ __forceinline__ float wave_sum(float x) {
  x += dpp<XOR1>(x); x += dpp<XOR2>(x); x += dpp<XOR7>(x); x += dpp<XOR8>(x);
  return x16_sum(x);
}
__device__ __forceinline__ float rg_sum(float x) { x += dpp<XOR8>(x); return x16_sum(x); }
__device__ __forceinline__ float sw32_add(float a, float b) {
  auto s = __builtin_amdgcn_permlane32_swap(__float_as_uint(a), __float_as_uint(b), false, false);
  return __uint_as_float(s[0]) + __uint_as_float(s[1]);
}
__device__ __forceinline__ float sw16_add(float a, float b) {
  auto s = __builtin_amdgcn_permlane16_swap(__float_as_uint(a), __float_as_uint(b), false, false);
  return __uint_as_float(s[0]) + __uint_as_float(s[1]);
}
__device__ __forceinline__ float tr_sum8(const float* v, bool b8) {
  float u[4], w[2];
#pragma unroll
  for (int k = 0; k < 4; ++k) u[k] = sw32_add(v[k], v[k + 4]);
#pragma unroll
  for (int k = 0; k < 2; ++k) w[k] = sw16_add(u[k], u[k + 2]);
  const float keep = b8 ? w[1] : w[0], send = b8 ? w[0] : w[1];
  return keep + dpp<XOR8>(send);
}
__device__ __forceinline__ float rdlane(float x, int l) { return __builtin_bit_cast(float, __builtin_amdgcn_readlane(__builtin_bit_cast(int, x), l)); }
#define LDNT(p) __builtin_nontemporal_load(reinterpret_cast<const u32x4*>(p))
#define WG_RLX __ATOMIC_RELAXED, __HIP_MEMORY_SCOPE_WORKGROUP
#define AG_RLX __ATOMIC_RELAXED, __HIP_MEMORY_SCOPE_AGENT
#define LLD(p) __builtin_amdgcn_readfirstlane(__hip_atomic_load((p), WG_RLX))
#define LDS_WAIT() asm volatile("s_waitcnt lgkmcnt(0)" ::: "memory")
constexpr int AUX_SC1 = 16;
constexpr int SPIN_CAP = 1 << 20;

#define STAMP(i) do {} while (0)

__device__ __forceinline__ void issue(u32x4 (&kr)[TS][3], u32x4 (&vr)[TS][3], const unsigned char* kp, const unsigned char* vp) {
#pragma unroll
  for (int t = 0; t < TS; ++t)
#pragma unroll
    for (int j = 0; j < 3; ++j) kr[t][j] = LDNT(kp + t * 3072 + j * 128);
#pragma unroll
  for (int t = 0; t < TS; ++t)
#pragma unroll
    for (int j = 0; j < 3; ++j) vr[t][j] = LDNT(vp + t * 3072 + j * 128);
}
__device__ __forceinline__ void compute(const u32x4 (&kr)[TS][3], const u32x4 (&vr)[TS][3], const u32x4 (&q)[3], float& m, float& l, float (&acc)[24]) {
  constexpr float SC = 0.07216878364870322f, LOG2E = 1.4426950408889634f;
  float s[TS];
#pragma unroll
  for (int t = 0; t < TS; ++t) {
    float a0 = 0.f, a1 = 0.f, a2 = 0.f;
#pragma unroll
    for (int k = 0; k < 4; ++k) { a0 = dot2(kr[t][0][k], q[0][k], a0); a1 = dot2(kr[t][1][k], q[1][k], a1); a2 = dot2(kr[t][2][k], q[2][k], a2); }
    float a = (a0 + a1) + a2;
    a += dpp<XOR1>(a); a += dpp<XOR2>(a); a += dpp<XOR7>(a);
    s[t] = rbf(rbf(a) * SC) * LOG2E;
  }
  float mx = s[0];
#pragma unroll
  for (int t = 1; t < TS; ++t) mx = fmaxf(mx, s[t]);
  if (__any(mx > m)) {
    float mn = fmaxf(m, mx);
    mn = fmaxf(mn, dpp<XOR8>(mn)); mn = x16_max(mn);
    const float al = __builtin_amdgcn_exp2f(m - mn);
    l *= al;
#pragma unroll
    for (int e = 0; e < 24; ++e) acc[e] *= al;
    m = mn;
  }
#pragma unroll
  for (int t = 0; t < TS; ++t) {
    const float p = __builtin_amdgcn_exp2f(s[t] - m);
    l += p;
#pragma unroll
    for (int j = 0; j < 3; ++j)
#pragma unroll
      for (int k = 0; k < 4; ++k) {
        acc[j * 8 + 2 * k] = fmaf(p, bflo(vr[t][j][k]), acc[j * 8 + 2 * k]);
        acc[j * 8 + 2 * k + 1] = fmaf(p, bfhi(vr[t][j][k]), acc[j * 8 + 2 * k + 1]);
      }
  }
}

template <bool B> struct BoolC { static constexpr bool value = B; };
__device__ __forceinline__ void put_g(__amdgpu_buffer_rsrc_t rs, int off, float a, float b, u32x2 tag) {
  u32x4 v = {__float_as_uint(a), __float_as_uint(b), tag[0], tag[1]};
  __builtin_amdgcn_raw_buffer_store_b128(v, rs, off, 0, AUX_SC1);
}
__device__ __forceinline__ bool tag_ok(u32x4 v, u32x2 tag) { return v[2] == tag[0] && v[3] == tag[1]; }
template <int N> __device__ __forceinline__ void poll_n(__amdgpu_buffer_rsrc_t rs, int off, int stride, u32x2 tag, u32x4 (&v)[N]) {
#pragma unroll 1
  for (int spin = 0; spin < SPIN_CAP; ++spin) {
    asm volatile("" : "+v"(off) :: "memory");
    bool ok = true;
#pragma unroll
    for (int i = 0; i < N; ++i) v[i] = __builtin_amdgcn_raw_buffer_load_b128(rs, off + i * stride, 0, AUX_SC1);
#pragma unroll
    for (int i = 0; i < N; ++i) ok = ok && tag_ok(v[i], tag);
    if (ok) break;
    __builtin_amdgcn_s_sleep(1);
  }
}

constexpr int L_W2 = 0;
constexpr int L_MRG = L_W2 + 64 * 256;
constexpr int L_Q = L_MRG + NW * 256 * 4;
constexpr int L_OV = L_Q + 96 * 4 + 64 * 4;
constexpr int L_RED = L_OV + 192 * 4;
constexpr int L_G = L_RED + NW * 128 * 4;
constexpr int L_CTL = L_G + 64;
constexpr int L_TAB = L_CTL + 64;
constexpr int L_END = L_TAB + QCAP * 4;
constexpr int L_BYTES = L_END > 84 * 1024 ? L_END : 84 * 1024;

__global__ void __launch_bounds__(NT, NW / 4) k_main(const unsigned char* __restrict__ Kc, const unsigned char* __restrict__ Vc,
                                                     const unsigned short* __restrict__ wq, const unsigned short* __restrict__ wo,
                                                     const unsigned short* __restrict__ w1, const unsigned short* __restrict__ b1,
                                                     const unsigned short* __restrict__ w2, const unsigned short* __restrict__ b2,
                                                     const unsigned short* __restrict__ x, unsigned char* ws, unsigned short* __restrict__ out) {
  __shared__ __attribute__((aligned(16))) unsigned char lds[L_BYTES];
  float* mrg = reinterpret_cast<float*>(lds + L_MRG);
  unsigned* lq = reinterpret_cast<unsigned*>(lds + L_Q);
  float* ov = reinterpret_cast<float*>(lds + L_OV);
  float* red = reinterpret_cast<float*>(lds + L_RED);
  unsigned* ctl = reinterpret_cast<unsigned*>(lds + L_CTL);
  unsigned* table = reinterpret_cast<unsigned*>(lds + L_TAB);
  const int tid = threadIdx.x, lane = tid & 63, wave = __builtin_amdgcn_readfirstlane(tid >> 6);
  const int r = lane >> 3, sub = lane & 7;
  const bool b8 = (lane & 8) != 0;
  const int bid = blockIdx.x;
  const int h = bid >> 3, sp = bid & 7;
  const __amdgpu_buffer_rsrc_t rs = __builtin_amdgcn_make_buffer_rsrc((void*)ws, 0, WS_END, 0x00020000);
  const unsigned long long tstart = __builtin_amdgcn_s_memrealtime();
  STAMP(0);

  const size_t base = ((size_t)h * SK + r) * (DH * 2) + sub * 16;
  const unsigned char* kp = Kc + base;
  const unsigned char* vp = Vc + base;
  u32x4 ka[TS][3], va[TS][3], kb[TS][3], vb[TS][3], kx_[NPRE - 2][TS][3], vx_[NPRE - 2][TS][3];
  u32x2 tag;
#define ROTH 0
#define ROTS 0
  const int rot = (h * ROTH + sp * ROTS) % SPT;
  auto stile = [&](int c) -> int { int t = c + rot; t = t >= SPT ? t - SPT : t; return sp * SPT + t; };
  const bool ffn = h < 24, prj = sp < 5;
  const bool two = wave < 2;
  auto prologue = [&](auto EXC) {
    constexpr bool EX = decltype(EXC)::value;
    if (EX) { if (sp == 0 && tid == 0) __builtin_amdgcn_raw_buffer_store_b32(0u, rs, WS_CNT + h * 256, 0, AUX_SC1); }
    unsigned xa[5];
#pragma unroll
    for (int i = 0; i < 5; ++i) xa[i] = reinterpret_cast<const unsigned*>(x)[lane + 64 * i];
    const int row0 = sp * 80 + r;
    const bool w1b = wave < 6;
    const int ga = wave, gb = EX ? wave + 8 : wave - 2, gc = (!EX && w1b) ? wave + 4 : gb;
    u32x4 wa[3], wb[3];
    {
      const unsigned char* p = reinterpret_cast<const unsigned char*>(wq) + ((size_t)(row0 + 8 * ga) * HD + h * DH) * 2 + sub * 16;
#pragma unroll
      for (int j = 0; j < 3; ++j) wa[j] = LDNT(p + j * 128);
    }
    if (EX) {
      const unsigned char* p = reinterpret_cast<const unsigned char*>(wq) + ((size_t)(row0 + 8 * gb) * HD + h * DH) * 2 + sub * 16;
#pragma unroll
      for (int j = 0; j < 3; ++j) wb[j] = LDNT(p + j * 128);
    } else {
      const unsigned char* p1 = reinterpret_cast<const unsigned char*>(w1);
      wb[0] = LDNT(p1 + (ffn ? ((size_t)(row0 + 8 * gb) * FF + h * 64) * 2 + sub * 16 : 0));
      wb[1] = LDNT(p1 + (ffn ? ((size_t)(row0 + 8 * gc) * FF + h * 64) * 2 + sub * 16 : 0));
    }
    const u32x2 ep = __builtin_amdgcn_raw_buffer_load_b64(rs, WS_EPOCH, 0, AUX_SC1);
    unsigned b1v = 0;
    if (EX) b1v = reinterpret_cast<const unsigned*>(b1)[ffn ? h * 32 + (lane & 31) : 0];
    __builtin_amdgcn_sched_barrier(0);
    STAMP(10);
    if (EX) {
      if (tid < 16) ctl[tid] = tid == 0 ? 6u * NPRE : (tid == 4 ? 32u : 0u);
      for (int i = tid; i < QCAP; i += 128) table[i] = 0;
    }
    float mb, den;
    {
      float s = 0.f;
#pragma unroll
      for (int i = 0; i < 5; ++i) s += bflo(xa[i]) + bfhi(xa[i]);
      s = wave_sum(s);
      const float mean = s * (1.f / DM);
      float q2 = 0.f;
#pragma unroll
      for (int i = 0; i < 5; ++i) { const float d0 = bflo(xa[i]) - mean, d1 = bfhi(xa[i]) - mean; q2 += d0 * d0 + d1 * d1; }
      q2 = wave_sum(q2);
      mb = rbf(mean);
      den = rbf(sqrtf(rbf(rbf(q2 * (1.f / DM)) + 1e-5f)));
    }
    auto xnorm = [&](int g) -> float {
      const int d0 = (sp * 80 + 8 * g) >> 1, rg = d0 >> 6;
      unsigned v = xa[0];
#pragma unroll
      for (int i = 1; i < 5; ++i) { v = rg == i ? xa[i] : v; asm volatile("" : "+v"(v)); }
      v = (unsigned)__builtin_amdgcn_ds_bpermute(((d0 & 63) + (r >> 1)) << 2, (int)v);
      const float xv = (r & 1) ? bfhi(v) : bflo(v);
      return rbf(rbf(xv - mb) / den);
    };
    u32x4 st[3];
    const int t6 = tid - 128;
    __builtin_amdgcn_sched_barrier(0);
    if (!EX) {
      const unsigned char* w2b = reinterpret_cast<const unsigned char*>(w2) + (size_t)h * (64 * DM * 2) + sp * 256;
#pragma unroll
      for (int k = 0; k < 3; ++k) {
        const int i = t6 + 384 * k;
        st[k] = LDNT((ffn && prj && i < 1024) ? w2b + (i >> 4) * (DM * 2) + (i & 15) * 16 : reinterpret_cast<const unsigned char*>(w2));
      }
      const size_t off = (size_t)stile(wave - 2) * (TR * DH * 2);
      issue(ka, va, kp + off, vp + off);
    }
    __builtin_amdgcn_sched_barrier(0);
    STAMP(12);
    {
      const float xna = xnorm(ga), xnb = xnorm(gb);
      float pa[24];
#pragma unroll
      for (int j = 0; j < 3; ++j)
#pragma unroll
        for (int k = 0; k < 4; ++k) { pa[j * 8 + 2 * k] = xna * bflo(wa[j][k]); pa[j * 8 + 2 * k + 1] = xna * bfhi(wa[j][k]); }
      if (EX) {
#pragma unroll
        for (int j = 0; j < 3; ++j)
#pragma unroll
          for (int k = 0; k < 4; ++k) { pa[j * 8 + 2 * k] = fmaf(xnb, bflo(wb[j][k]), pa[j * 8 + 2 * k]); pa[j * 8 + 2 * k + 1] = fmaf(xnb, bfhi(wb[j][k]), pa[j * 8 + 2 * k + 1]); }
      }
      float* rec = mrg + wave * 256 + sub * 8 + r;
#pragma unroll
      for (int j = 0; j < 3; ++j) rec[j * 64] = tr_sum8(pa + j * 8, b8);
      float pz = 0.f;
      if (!EX) {
        float pb[8];
        const float xc = w1b ? xnorm(gc) : 0.f;
#pragma unroll
        for (int k = 0; k < 4; ++k) { pb[2 * k] = fmaf(xc, bflo(wb[1][k]), xnb * bflo(wb[0][k])); pb[2 * k + 1] = fmaf(xc, bfhi(wb[1][k]), xnb * bfhi(wb[0][k])); }
        pz = tr_sum8(pb, b8);
      }
      rec[192] = pz;
    }
    { const unsigned long long e = ((unsigned long long)ep[1] << 32 | ep[0]) + 1ull; tag[0] = __builtin_amdgcn_readfirstlane((unsigned)e); tag[1] = __builtin_amdgcn_readfirstlane((unsigned)(e >> 32)); }
    __builtin_amdgcn_sched_barrier(0);
    if (!EX) { const size_t off2 = (size_t)stile(6 + wave - 2) * (TR * DH * 2); issue(kb, vb, kp + off2, vp + off2); }
    __builtin_amdgcn_sched_barrier(0);
    LDS_WAIT();
    __builtin_amdgcn_s_barrier();
    asm volatile("" ::: "memory");
    STAMP(1);
    if (EX) {
      {
        float s0 = 0.f, s1 = 0.f;
#pragma unroll
        for (int w = 0; w < NW; ++w) { const f32x2 v = *reinterpret_cast<const f32x2*>(mrg + w * 256 + 2 * tid); s0 += v[0]; s1 += v[1]; }
        asm volatile("s_waitcnt vmcnt(0)" ::: "memory");
        put_g(rs, WS_QX + bid * 2048 + tid * 16, s0, s1, tag);
      }
      STAMP(2);
      {
        u32x4 v[8];
        poll_n<8>(rs, WS_QX + h * 8 * 2048 + tid * 16, 2048, tag, v);
        float s0 = 0.f, s1 = 0.f;
#pragma unroll
        for (int i = 0; i < 8; ++i) { s0 += __uint_as_float(v[i][0]); s1 += __uint_as_float(v[i][1]); }
        if (tid < 96) lq[tid] = (unsigned)f2bf(s0) | ((unsigned)f2bf(s1) << 16);
        LDS_WAIT();
        if (lane == 0) __hip_atomic_fetch_add(&ctl[5], 1u, WG_RLX);
        if (wave == 1) {
          float g[2];
#pragma unroll
          for (int k = 0; k < 2; ++k) {
            const float hv = rbf(rbf(k ? s1 : s0) + (k ? bfhi(b1v) : bflo(b1v)));
            const float u = 0.7978845608028654f * (hv + 0.044715f * hv * hv * hv);
            g[k] = rbf(0.5f * hv * (1.f + tanhf(u)));
          }
          if (tid >= 96) *reinterpret_cast<f32x2*>(lq + 96 + 2 * (tid - 96)) = f32x2{g[0], g[1]};
          LDS_WAIT();
          if (lane == 0) __hip_atomic_fetch_add(&ctl[6], 1u, WG_RLX);
        }
      }
      STAMP(14);
    } else {
      *reinterpret_cast<u32x4*>(lds + L_W2 + t6 * 16) = st[0];
      *reinterpret_cast<u32x4*>(lds + L_W2 + (t6 + 384) * 16) = st[1];
      if (t6 < 256) *reinterpret_cast<u32x4*>(lds + L_W2 + (t6 + 768) * 16) = st[2];
      LDS_WAIT();
      if (lane == 0) __hip_atomic_fetch_add(&ctl[6], 1u, WG_RLX);
      const unsigned long long ta = tstart + PACE0 + PACED * (wave - 2);
      auto pace = [&](unsigned long long t) {
#pragma unroll 1
        for (int spin = 0; spin < SPIN_CAP; ++spin) { if (__builtin_amdgcn_s_memrealtime() >= t || LLD(&ctl[5]) >= 2u) break; __builtin_amdgcn_s_sleep(1); }
      };
#pragma unroll
      for (int i = 0; i < NPRE - 2; ++i) {
        pace(ta + PACED * 6 * i);
        const size_t off = (size_t)stile(12 + 6 * i + wave - 2) * (TR * DH * 2);
        issue(kx_[i], vx_[i], kp + off, vp + off);
      }
    }
  };
  if (two) prologue(BoolC<true>{}); else prologue(BoolC<false>{});
#pragma unroll 1
  for (int spin = 0; spin < SPIN_CAP; ++spin) { if (LLD(&ctl[5]) >= 2u) break; __builtin_amdgcn_s_sleep(1); }
  asm volatile("" ::: "memory");
  u32x4 q[3];
#pragma unroll
  for (int j = 0; j < 3; ++j) q[j] = *reinterpret_cast<const u32x4*>(lq + j * 32 + sub * 4);
  STAMP(3);

  unsigned* cnt = reinterpret_cast<unsigned*>(ws + WS_CNT) + h * 64;
  unsigned zoff = 0; asm volatile("" : "+v"(zoff));
  float m = -INFINITY, l = 0.f, acc[24];
#pragma unroll
  for (int e = 0; e < 24; ++e) acc[e] = 0.f;

  unsigned long long tlast = 0, tcomp = 0;
  int cur;
  const unsigned lds0 = (unsigned)(size_t)(__attribute__((address_space(3))) unsigned char*)lds;
  auto deliver = [&](unsigned gx) {
    const int g = __builtin_amdgcn_readfirstlane(gx);
    if ((unsigned)g < (unsigned)NCH) {
      int sz, st;
      if (g < G32) { sz = 32; st = g * 32; }
      else if (g < G16) { sz = 16; st = S16 + (g - G32) * 16; }
      else if (g < G8) { sz = 8; st = S8 + (g - G16) * 8; }
      else if (g < G4) { sz = 4; st = S8 + N8 * 8 + (g - G8) * 4; }
      else if (g < G2) { sz = 2; st = S8 + N8 * 8 + N4 * 4 + (g - G4) * 2; }
      else { sz = 1; st = S8 + N8 * 8 + N4 * 4 + N2 * 2 + (g - G2); }
      unsigned p = 0;
      if (lane == 0) { p = __hip_atomic_fetch_add(&ctl[3], (unsigned)sz, WG_RLX); __hip_atomic_fetch_min(&ctl[4], (unsigned)sz, WG_RLX); }
      p = __builtin_amdgcn_readfirstlane(p);
      int u = st + lane;
      { const int b = u >> 5; u = (((b % NSUB) * (POOL / 32 / NSUB) + b / NSUB) << 5) + (u & 31); }
      if (lane < sz) __hip_atomic_store(&table[p + lane], (unsigned)(DYN0 + u + 1), WG_RLX);
    } else if (lane == 0) __hip_atomic_store(&ctl[1], 1u, WG_RLX);
    LDS_WAIT();
    if (lane == 0) __hip_atomic_fetch_add(&ctl[2], ~0u, WG_RLX);
  };
  auto claim = [&](unsigned seen) -> bool {
    unsigned s2 = 0;
    if (lane == 0) {
      const unsigned old = __hip_atomic_fetch_add(&ctl[2], 1u, WG_RLX);
      LDS_WAIT();
      s2 = __hip_atomic_load(&ctl[1], WG_RLX) | (old > seen ? 1u : 0u);
      if (s2) __hip_atomic_fetch_add(&ctl[2], ~0u, WG_RLX);
    }
    return __builtin_amdgcn_readfirstlane(s2) == 0;
  };
  int ntile;
  bool pend = false;
  unsigned gx = 0;
  unsigned stp, outs, nq, lsz, tqv;
  auto snap = [&](int idx) {
    u32x4 c4; unsigned l4, tq;
    const unsigned qaddr = lds0 + L_TAB + (unsigned)(idx > 0 ? idx : 0) * 4u;
    asm volatile("ds_read_b128 %0, %3 offset:%5\n\tds_read_b32 %1, %3 offset:%6\n\tds_read_b32 %2, %4\n\ts_waitcnt lgkmcnt(0)"
                 : "=&v"(c4), "=&v"(l4), "=&v"(tq) : "v"(lds0), "v"(qaddr), "i"(L_CTL), "i"(L_CTL + 16) : "memory");
    stp = __builtin_amdgcn_readfirstlane(c4[1]); outs = __builtin_amdgcn_readfirstlane(c4[2]); nq = __builtin_amdgcn_readfirstlane(c4[3]);
    lsz = __builtin_amdgcn_readfirstlane(l4); tqv = __builtin_amdgcn_readfirstlane(tq);
  };
  auto prep = [&]() {
    unsigned nx = 0;
    if (lane == 0) nx = __hip_atomic_fetch_add(&ctl[0], 1u, WG_RLX);
    cur = __builtin_amdgcn_readfirstlane(nx);
    ntile = stile(cur);
    if (cur >= SPT - LOWW - NW) {
      const int idx = cur - SPT;
      snap(idx);
      if (idx >= 0) ntile = (int)tqv - 1;
      const int level = (int)nq + (int)(outs * lsz) - (idx + 1);
      if (stp == 0 && level < (lsz <= 2u ? LOWE : (lsz <= 4u ? LOW4 : (lsz <= 8u ? LOW8 : LOWW))) && claim(outs)) {
        if (lane == 0) gx = __hip_atomic_fetch_add(cnt + zoff, 1u, AG_RLX);
        pend = true;
      }
    }
  };
  auto fetch2 = [&](u32x4 (&kx)[TS][3], u32x4 (&vx)[TS][3]) -> bool {
    if (ntile < 0) {
      if (pend) { deliver(gx); pend = false; }
      const int idx = cur - SPT;
#pragma unroll 1
      for (int spin = 0;; ++spin) {
        snap(idx);
        if (tqv != 0) { ntile = (int)tqv - 1; break; }
        if (spin >= SPIN_CAP) return false;
        if (outs == 0 && nq <= (unsigned)idx) {
          if (stp != 0) return false;
          if (claim(0u)) {
            unsigned g2 = 0;
            if (lane == 0) g2 = __hip_atomic_fetch_add(cnt + zoff, 1u, AG_RLX);
            deliver(g2);
          }
        } else __builtin_amdgcn_s_sleep(1);
      }
    }
    const size_t off = (size_t)ntile * (TR * DH * 2);
    issue(kx, vx, kp + off, vp + off);
    __builtin_amdgcn_sched_barrier(0);
    if (pend) { deliver(gx); pend = false; }
    prep();
    return true;
  };
  prep();
  bool has;
  if (!two) {
    compute(ka, va, q, m, l, acc);
    has = fetch2(ka, va);
    compute(kb, vb, q, m, l, acc);
#pragma unroll
    for (int i = 0; i < NPRE - 2; ++i) compute(kx_[i], vx_[i], q, m, l, acc);
  } else {
    has = fetch2(ka, va);
    if (wave == 0 && ffn && prj) {
#pragma unroll 1
      for (int spin = 0; spin < SPIN_CAP; ++spin) { if (LLD(&ctl[6]) >= 7u) break; __builtin_amdgcn_s_sleep(1); }
      asm volatile("" ::: "memory");
      const float* hv = reinterpret_cast<const float*>(lq + 96);
      float z0 = 0.f, z1 = 0.f;
#pragma unroll 8
      for (int i = 0; i < 64; ++i) { const unsigned wv = *reinterpret_cast<const unsigned*>(lds + L_W2 + i * 256 + lane * 4); const float hh = hv[i]; z0 = fmaf(hh, bflo(wv), z0); z1 = fmaf(hh, bfhi(wv), z1); }
      put_g(rs, WS_ZX + (sp * 8 + (lane >> 3)) * (NH * 128) + h * 128 + (lane & 7) * 16, z0, z1, tag);
    }
  }
  while (has) {
    compute(ka, va, q, m, l, acc);
    has = fetch2(ka, va);
  }
  STAMP(4);

  const int own = h * 3 + (sp - 5);
  const bool owner = !prj && own < NOWN;
  int tk;
  {
    const float M = m;
    const float L = rg_sum(l);
    float* rec = mrg + wave * PREC;
#pragma unroll
    for (int j = 0; j < 3; ++j) rec[j * 64 + lane] = tr_sum8(acc + j * 8, b8);
    if (lane == 0) *reinterpret_cast<f32x2*>(rec + 192) = f32x2{M, L};
    LDS_WAIT();
    unsigned t = 0;
    if (lane == 0) t = __hip_atomic_fetch_add(&ctl[7], 1u, WG_RLX);
    tk = __builtin_amdgcn_readfirstlane(t);
    asm volatile("" ::: "memory");
  }
  STAMP(11);
  if (tk == NW - 1) {
    f32x2 ml[NW];
    float p[NW][3];
#pragma unroll
    for (int w = 0; w < NW; ++w) {
      ml[w] = *reinterpret_cast<const f32x2*>(mrg + w * PREC + 192);
#pragma unroll
      for (int j = 0; j < 3; ++j) p[w][j] = mrg[w * PREC + j * 64 + lane];
    }
    float MM = ml[0][0];
#pragma unroll
    for (int w = 1; w < NW; ++w) MM = fmaxf(MM, ml[w][0]);
    float L = 0.f, o[3] = {0.f, 0.f, 0.f};
#pragma unroll
    for (int w = 0; w < NW; ++w) {
      const float wt = __builtin_amdgcn_exp2f(ml[w][0] - MM);
      L = fmaf(wt, ml[w][1], L);
#pragma unroll
      for (int j = 0; j < 3; ++j) o[j] = fmaf(wt, p[w][j], o[j]);
    }
    const int off = WS_PX + bid * 2048 + sub * 256 + r * 32;
    put_g(rs, off, o[0], o[1], tag);
    put_g(rs, off + 16, o[2], b8 ? L : MM, tag);
    if (lane == 0) __hip_atomic_store(&ctl[8], 1u, WG_RLX);
    STAMP(5);
  }
  __builtin_amdgcn_sched_barrier(0);
  if (prj) {
    u32x4 wreg[6];
    {
      const unsigned char* wp = reinterpret_cast<const unsigned char*>(wo) + (size_t)(h * DH + wave * 4 + (lane >> 4)) * (DM * 2) + sp * 256 + (lane & 15) * 16;
#pragma unroll
      for (int k = 0; k < 6; ++k) wreg[k] = LDNT(wp + (size_t)k * 32 * (DM * 2));
    }
    f32x2 wf[6][4];
#pragma unroll 1
    for (int spin = 0; spin < SPIN_CAP; ++spin) { if (LLD(&ctl[8]) != 0) break; __builtin_amdgcn_s_sleep(1); }
    float oa, ob;
    {
      const int gi = lane & 15, pg = lane >> 4;
      int off = WS_PX + (h * 8 + pg) * 2048 + (((gi >> 3) * 32 + wave * 4 + ((gi >> 1) & 3)) * 2 + (gi & 1)) * 16;
      u32x4 va_ = __builtin_amdgcn_raw_buffer_load_b128(rs, off, 0, AUX_SC1), vb_ = __builtin_amdgcn_raw_buffer_load_b128(rs, off + 4 * 2048, 0, AUX_SC1);
      __builtin_amdgcn_sched_barrier(0);
#pragma unroll
      for (int k = 0; k < 6; ++k)
#pragma unroll
        for (int e = 0; e < 4; ++e) { wf[k][e] = f32x2{bflo(wreg[k][e]), bfhi(wreg[k][e])}; asm volatile("" : "+v"(wf[k][e])); }
#pragma unroll 1
      for (int spin = 0; spin < SPIN_CAP; ++spin) {
        asm volatile("" : "+v"(off) :: "memory");
        const bool na = !tag_ok(va_, tag), nb = !tag_ok(vb_, tag);
        if (!na && !nb) break;
        if (na) va_ = __builtin_amdgcn_raw_buffer_load_b128(rs, off, 0, AUX_SC1);
        if (nb) vb_ = __builtin_amdgcn_raw_buffer_load_b128(rs, off + 4 * 2048, 0, AUX_SC1);
      }
      STAMP(15);
      const int rowb = (lane & 48) << 2;
      const float Ma = __builtin_bit_cast(float, __builtin_amdgcn_ds_bpermute(rowb + 4, (int)va_[1])), La = __builtin_bit_cast(float, __builtin_amdgcn_ds_bpermute(rowb + 12, (int)va_[1]));
      const float Mb = __builtin_bit_cast(float, __builtin_amdgcn_ds_bpermute(rowb + 4, (int)vb_[1])), Lb = __builtin_bit_cast(float, __builtin_amdgcn_ds_bpermute(rowb + 12, (int)vb_[1]));
      const float MM = x16_max(fmaxf(Ma, Mb));
      const float wa_ = __builtin_amdgcn_exp2f(Ma - MM), wb_ = __builtin_amdgcn_exp2f(Mb - MM);
      const float L = x16_sum(fmaf(wa_, La, wb_ * Lb));
      const float s0 = x16_sum(fmaf(wa_, __uint_as_float(va_[0]), wb_ * __uint_as_float(vb_[0])));
      const float s1 = x16_sum(fmaf(wa_, __uint_as_float(va_[1]), wb_ * __uint_as_float(vb_[1])));
      const float inv = 1.f / L;
      oa = rbf(s0 * inv); ob = rbf(s1 * inv);
    }
    STAMP(6);
    f32x2 y2[4];
#pragma unroll
    for (int e = 0; e < 4; ++e) y2[e] = f32x2{0.f, 0.f};
#pragma unroll
    for (int k = 0; k < 6; ++k) {
      const int src = (((k & 1) * 8 + (lane >> 4) * 2 + (k >= 4 ? 1 : 0)) << 2);
      const float o = __builtin_bit_cast(float, __builtin_amdgcn_ds_bpermute(src, __builtin_bit_cast(int, (k == 2 || k == 3) ? ob : oa)));
      const f32x2 o2 = f32x2{o, o};
#pragma unroll
      for (int e = 0; e < 4; ++e) y2[e] = __builtin_elementwise_fma(o2, wf[k][e], y2[e]);
    }
    float u[4];
    u[0] = sw32_add(y2[0][0], y2[2][0]); u[1] = sw32_add(y2[0][1], y2[2][1]); u[2] = sw32_add(y2[1][0], y2[3][0]); u[3] = sw32_add(y2[1][1], y2[3][1]);
    const float w0 = sw16_add(u[0], u[2]), w1v = sw16_add(u[1], u[3]);
    *reinterpret_cast<f32x2*>(red + wave * 128 + (lane & 15) * 8 + (lane >> 4) * 2) = f32x2{w0, w1v};
    LDS_WAIT();
    unsigned t = 0;
    if (lane == 0) t = __hip_atomic_fetch_add(&ctl[10], 1u, WG_RLX);
    const int tk2 = __builtin_amdgcn_readfirstlane(t);
    asm volatile("" ::: "memory");
    if (tk2 == NW - 1) {
      float y0 = 0.f, y1 = 0.f;
#pragma unroll
      for (int w = 0; w < NW; ++w) { const f32x2 v = *reinterpret_cast<const f32x2*>(red + w * 128 + lane * 2); y0 += v[0]; y1 += v[1]; }
      put_g(rs, WS_YX + (sp * 8 + (lane >> 3)) * (NH * 128) + h * 128 + (lane & 7) * 16, y0, y1, tag);
      STAMP(7);
    }
    return;
  }
  if (!owner || tk != 0) return;
  {
    const unsigned xv = reinterpret_cast<const unsigned*>(x)[own * 8 + (lane & 7)];
    const unsigned bv = reinterpret_cast<const unsigned*>(b2)[own * 8 + (lane & 7)];
    float g0, g1;
    {
      u32x4 v[3];
      poll_n<3>(rs, WS_ZX + own * (NH * 128) + lane * 16, 1024, tag, v);
      float z0 = (__uint_as_float(v[0][0]) + __uint_as_float(v[1][0])) + __uint_as_float(v[2][0]);
      float z1 = (__uint_as_float(v[0][1]) + __uint_as_float(v[1][1])) + __uint_as_float(v[2][1]);
      z0 = rg_sum(z0); z1 = rg_sum(z1);
      z0 = rbf(rbf(z0) + bflo(bv)); z1 = rbf(rbf(z1) + bfhi(bv));
      g0 = rbf(z0 / (1.f + __expf(-z0))); g1 = rbf(z1 / (1.f + __expf(-z1)));
    }
#pragma unroll 1
    for (int spin = 0; spin < SPIN_CAP; ++spin) { if (LLD(&ctl[8]) != 0) break; __builtin_amdgcn_s_sleep(1); }
    u32x4 v[4];
#pragma unroll
    for (int i = 0; i < 4; ++i) v[i] = u32x4{0u, 0u, ~tag[0], 0u};
    int off = WS_YX + own * (NH * 128) + lane * 16;
#pragma unroll 1
    for (int spin = 0; spin < SPIN_CAP; ++spin) {
      asm volatile("" : "+v"(off) :: "memory");
      bool any = false;
#pragma unroll
      for (int i = 0; i < 4; ++i) { const bool nd = !tag_ok(v[i], tag); any = any || nd; if (nd) v[i] = __builtin_amdgcn_raw_buffer_load_b128(rs, off + i * 1024, 0, AUX_SC1); }
      if (!any) break;
    }
    STAMP(8);
    float s0 = (__uint_as_float(v[0][0]) + __uint_as_float(v[1][0])) + (__uint_as_float(v[2][0]) + __uint_as_float(v[3][0]));
    float s1 = (__uint_as_float(v[0][1]) + __uint_as_float(v[1][1])) + (__uint_as_float(v[2][1]) + __uint_as_float(v[3][1]));
    s0 = rg_sum(s0); s1 = rg_sum(s1);
    if (lane < 8) {
      const float o0 = bflo(xv) + rbf(rbf(s0) * g0), o1 = bfhi(xv) + rbf(rbf(s1) * g1);
      reinterpret_cast<unsigned*>(out)[own * 8 + lane] = (unsigned)f2bf(o0) | ((unsigned)f2bf(o1) << 16);
    }
    STAMP(9);
    if (own == 0 && lane == 0) __builtin_amdgcn_raw_buffer_store_b64(tag, rs, WS_EPOCH, 0, AUX_SC1);
  }
}

extern "C" void kernel_launch(void* const* d_in, const int* in_sizes, int n_in,
                              void* d_out, int out_size, void* d_ws, size_t ws_size,
                              hipStream_t stream) {
  const unsigned short* x = (const unsigned short*)d_in[0];
  const unsigned char* kc = (const unsigned char*)d_in[1];
  const unsigned char* vc = (const unsigned char*)d_in[2];
  const unsigned short* wq = (const unsigned short*)d_in[3];
  const unsigned short* wo = (const unsigned short*)d_in[4];
  const unsigned short* w1 = (const unsigned short*)d_in[6];
  const unsigned short* b1 = (const unsigned short*)d_in[7];
  const unsigned short* w2 = (const unsigned short*)d_in[8];
  const unsigned short* b2 = (const unsigned short*)d_in[9];
  unsigned char* ws = (unsigned char*)d_ws;
  k_main<<<NWG, NT, 0, stream>>>(kc, vc, wq, wo, w1, b1, w2, b2, x, ws, (unsigned short*)d_out);
}
```

```cpp
#include <hip/hip_runtime.h>
#include <hip/hip_bf16.h>
#include <stdint.h>

constexpr int DM = 640, NH = 32, SK = 32768, DH = 192, FF = 1536, HD = NH * DH;
constexpr int NW = 8, NT = NW * 64;
constexpr int NSPL = 8, NWG = NH * NSPL;
constexpr int TBYTES = 8 * DH * 2;
constexpr int NTILE = SK / 8;
#define SPT 344
#define PACE0 400
#define PACED 22
#define NPRE 5
#define LOWW 10
#define NSUB 2
#define N32 18
#define N8 16
#define N4 32
constexpr int DYN0 = NSPL * SPT, POOL = NTILE - DYN0;
constexpr int N16 = (POOL - 32 * N32 - 8 * N8 - 4 * N4) / 16;
static_assert(N32 * 32 + N16 * 16 + 8 * N8 + 4 * N4 == POOL && N16 >= 0, "chunk table covers the pool");
static_assert(POOL % (32 * NSUB) == 0, "");
static_assert(6 * NPRE + NW < SPT - LOWW - NW, "");
constexpr int G32 = N32, G16 = G32 + N16, G8 = G16 + N8, NCH = G8 + N4;
constexpr int S16 = 32 * N32, S8 = S16 + 16 * N16, S4 = S8 + 8 * N8;
constexpr int QCAP = POOL + 64;
constexpr int PREC = 200;
constexpr int NOWN = 40;

constexpr int WS_EPOCH = 0;
constexpr int WS_CNT = 4096;
constexpr int WS_QX = 16384;
constexpr int WS_PX = WS_QX + NWG * 2048;
constexpr int WS_YX = WS_PX + NWG * 4096;
constexpr int WS_ZX = WS_YX + NOWN * NH * 128;
constexpr int WS_END = WS_ZX + NOWN * NH * 128;

constexpr int L_W2 = 0;
constexpr int L_MRG = L_W2 + 64 * 256;
constexpr int L_Q = L_MRG + NW * PREC * 8;
constexpr int L_RED = L_Q + 160 * 4;
constexpr int L_CTL = L_RED + NW * 128 * 4;
constexpr int L_TAB = L_CTL + 64;
constexpr int L_END = L_TAB + QCAP * 4;
constexpr int L_BYTES = L_END > 84 * 1024 ? L_END : 84 * 1024;
static_assert(L_CTL % 16 == 0, "");
constexpr int C_TICK = 0, C_STOP = 1, C_OUTS = 2, C_NQ = 3, C_LSZ = 4, C_QRDY = 5, C_FFN = 6, C_REC = 7, C_PUB = 8, C_Y = 9;

typedef unsigned u32x4 __attribute__((ext_vector_type(4)));
typedef unsigned u32x2 __attribute__((ext_vector_type(2)));
typedef float f32x2 __attribute__((ext_vector_type(2)));
typedef __bf16 bf16x2 __attribute__((ext_vector_type(2)));

__device__ __forceinline__ float bflo(unsigned u) { return __builtin_bit_cast(float, u << 16); }
__device__ __forceinline__ float bfhi(unsigned u) { return __builtin_bit_cast(float, u & 0xffff0000u); }
__device__ __forceinline__ float rbf(float x) { return (float)(__bf16)x; }
__device__ __forceinline__ unsigned pk2(float a, float b) {
  return (unsigned)__builtin_bit_cast(unsigned short, (__bf16)a) | ((unsigned)__builtin_bit_cast(unsigned short, (__bf16)b) << 16);
}
__device__ __forceinline__ float dot2(unsigned k, unsigned q, float acc) {
  return __builtin_amdgcn_fdot2_f32_bf16(__builtin_bit_cast(bf16x2, k), __builtin_bit_cast(bf16x2, q), acc, false);
}
template <int CTRL> __device__ __forceinline__ float dpp(float x) {
  return __builtin_bit_cast(float, __builtin_amdgcn_mov_dpp(__builtin_bit_cast(int, x), CTRL, 0xf, 0xf, true));
}
constexpr int XOR1 = 0xB1, XOR2 = 0x4E, XOR7 = 0x141, XOR8 = 0x128;
__device__ __forceinline__ float sw32_add(float a, float b) {
  auto s = __builtin_amdgcn_permlane32_swap(__float_as_uint(a), __float_as_uint(b), false, false);
  return __uint_as_float(s[0]) + __uint_as_float(s[1]);
}
__device__ __forceinline__ float sw16_add(float a, float b) {
  auto s = __builtin_amdgcn_permlane16_swap(__float_as_uint(a), __float_as_uint(b), false, false);
  return __uint_as_float(s[0]) + __uint_as_float(s[1]);
}
__device__ __forceinline__ float x32_max(float x) {
  auto t = __builtin_amdgcn_permlane32_swap(__float_as_uint(x), __float_as_uint(x), false, false);
  return fmaxf(__uint_as_float(t[0]), __uint_as_float(t[1]));
}
__device__ __forceinline__ float x16_max(float x) {
  auto s = __builtin_amdgcn_permlane16_swap(__float_as_uint(x), __float_as_uint(x), false, false);
  return x32_max(fmaxf(__uint_as_float(s[0]), __uint_as_float(s[1])));
}
__device__ __forceinline__ float rg_sum(float x) {
  x += dpp<XOR8>(x); x = sw16_add(x, x); return sw32_add(x, x);
}
__device__ __forceinline__ float wave_sum(float x) { x += dpp<XOR1>(x); x += dpp<XOR2>(x); x += dpp<XOR7>(x); return rg_sum(x); }
__device__ __forceinline__ float tr_sum8(const float* v, bool b8) {
  float u[4], w[2];
#pragma unroll
  for (int k = 0; k < 4; ++k) u[k] = sw32_add(v[k], v[k + 4]);
#pragma unroll
  for (int k = 0; k < 2; ++k) w[k] = sw16_add(u[k], u[k + 2]);
  const float keep = b8 ? w[1] : w[0], send = b8 ? w[0] : w[1];
  return keep + dpp<XOR8>(send);
}
__device__ __forceinline__ double mkd(unsigned lo, unsigned hi) { return __builtin_bit_cast(double, u32x2{lo, hi}); }
__device__ __forceinline__ double sw32_addd(double a, double b) {
  const u32x2 x = __builtin_bit_cast(u32x2, a), y = __builtin_bit_cast(u32x2, b);
  auto lo = __builtin_amdgcn_permlane32_swap(x[0], y[0], false, false); auto hi = __builtin_amdgcn_permlane32_swap(x[1], y[1], false, false);
  return mkd(lo[0], hi[0]) + mkd(lo[1], hi[1]);
}
__device__ __forceinline__ double sw16_addd(double a, double b) {
  const u32x2 x = __builtin_bit_cast(u32x2, a), y = __builtin_bit_cast(u32x2, b);
  auto lo = __builtin_amdgcn_permlane16_swap(x[0], y[0], false, false); auto hi = __builtin_amdgcn_permlane16_swap(x[1], y[1], false, false);
  return mkd(lo[0], hi[0]) + mkd(lo[1], hi[1]);
}
__device__ __forceinline__ double ror8d(double a) {
  const u32x2 x = __builtin_bit_cast(u32x2, a);
  return mkd((unsigned)__builtin_amdgcn_mov_dpp((int)x[0], XOR8, 0xf, 0xf, true), (unsigned)__builtin_amdgcn_mov_dpp((int)x[1], XOR8, 0xf, 0xf, true));
}
__device__ __forceinline__ double tr_sum8d(const double* v, bool b8) {
  double u[4], w[2];
#pragma unroll
  for (int k = 0; k < 4; ++k) u[k] = sw32_addd(v[k], v[k + 4]);
#pragma unroll
  for (int k = 0; k < 2; ++k) w[k] = sw16_addd(u[k], u[k + 2]);
  const double keep = b8 ? w[1] : w[0], send = b8 ? w[0] : w[1];
  return keep + ror8d(send);
}
__device__ __forceinline__ double rg_sumd(double x) { x += ror8d(x); x = sw16_addd(x, x); return sw32_addd(x, x); }
__device__ __forceinline__ int shift_of(float d) { return (int)fmaxf(d, -4000.f); }

#define LDNT(p) __builtin_nontemporal_load(reinterpret_cast<const u32x4*>(p))
#define WG_RLX __ATOMIC_RELAXED, __HIP_MEMORY_SCOPE_WORKGROUP
#define AG_RLX __ATOMIC_RELAXED, __HIP_MEMORY_SCOPE_AGENT
#define LLD(p) __builtin_amdgcn_readfirstlane(__hip_atomic_load((p), WG_RLX))
#define LDS_WAIT() asm volatile("s_waitcnt lgkmcnt(0)" ::: "memory")
#define CBAR() asm volatile("" ::: "memory")
#define LDS_SPIN(cond) do { _Pragma("unroll 1") for (int spin_ = 0; spin_ < SPIN_CAP; ++spin_) { if (cond) break; __builtin_amdgcn_s_sleep(1); } CBAR(); } while (0)
constexpr int AUX_SC1 = 16;
constexpr int SPIN_CAP = 1 << 20;

#define STAMP(i) do {} while (0)

struct Tile { u32x4 k[3], v[3]; };
__device__ __forceinline__ void issue(Tile& t, const unsigned char* kp, const unsigned char* vp, int tile) {
  const size_t off = (size_t)tile * TBYTES;
#pragma unroll
  for (int j = 0; j < 3; ++j) t.k[j] = LDNT(kp + off + j * 128);
#pragma unroll
  for (int j = 0; j < 3; ++j) t.v[j] = LDNT(vp + off + j * 128);
}
__device__ __forceinline__ void compute(const Tile& t, const u32x4 (&q)[3], float& m, double& l, double (&acc)[24]) {
  constexpr float SC = 0.07216878364870322f, LOG2E = 1.4426950408889634f;
  float a0 = 0.f, a1 = 0.f, a2 = 0.f;
#pragma unroll
  for (int k = 0; k < 4; ++k) { a0 = dot2(t.k[0][k], q[0][k], a0); a1 = dot2(t.k[1][k], q[1][k], a1); a2 = dot2(t.k[2][k], q[2][k], a2); }
  float a = (a0 + a1) + a2;
  a += dpp<XOR1>(a); a += dpp<XOR2>(a); a += dpp<XOR7>(a);
  const float s = rbf(rbf(a) * SC) * LOG2E;
  const float fl = __builtin_floorf(s);
  if (__any(fl > m)) {
    float mn = fmaxf(m, fl);
    mn = fmaxf(mn, dpp<XOR8>(mn)); mn = x16_max(mn);
    const int sh = shift_of(m - mn);
    l = __builtin_amdgcn_ldexp(l, sh);
#pragma unroll
    for (int e = 0; e < 24; ++e) acc[e] = __builtin_amdgcn_ldexp(acc[e], sh);
    m = mn;
  }
  const double p = __builtin_amdgcn_ldexp((double)__builtin_amdgcn_exp2f(s - fl), shift_of(fl - m));
  l += p;
#pragma unroll
  for (int j = 0; j < 3; ++j)
#pragma unroll
    for (int k = 0; k < 4; ++k) {
      acc[j * 8 + 2 * k] = __builtin_fma(p, (double)bflo(t.v[j][k]), acc[j * 8 + 2 * k]);
      acc[j * 8 + 2 * k + 1] = __builtin_fma(p, (double)bfhi(t.v[j][k]), acc[j * 8 + 2 * k + 1]);
    }
}

__device__ __forceinline__ void put_g(__amdgpu_buffer_rsrc_t rs, int off, float a, float b, u32x2 tag) {
  const u32x4 v = {__float_as_uint(a), __float_as_uint(b), tag[0], tag[1]};
  __builtin_amdgcn_raw_buffer_store_b128(v, rs, off, 0, AUX_SC1);
}
__device__ __forceinline__ void put_gd(__amdgpu_buffer_rsrc_t rs, int off, double a, u32x2 tag) {
  const u32x2 x = __builtin_bit_cast(u32x2, a);
  const u32x4 v = {x[0], x[1], tag[0], tag[1]};
  __builtin_amdgcn_raw_buffer_store_b128(v, rs, off, 0, AUX_SC1);
}
__device__ __forceinline__ u32x4 get_g(__amdgpu_buffer_rsrc_t rs, int off) { return __builtin_amdgcn_raw_buffer_load_b128(rs, off, 0, AUX_SC1); }
__device__ __forceinline__ bool tag_ok(u32x4 v, u32x2 tag) { return v[2] == tag[0] && v[3] == tag[1]; }
template <int N> __device__ __forceinline__ void poll(__amdgpu_buffer_rsrc_t rs, int off, int stride, u32x2 tag, u32x4 (&v)[N]) {
#pragma unroll 1
  for (int spin = 0; spin < SPIN_CAP; ++spin) {
    asm volatile("" : "+v"(off) :: "memory");
    bool any = false;
#pragma unroll
    for (int i = 0; i < N; ++i) { const bool nd = !tag_ok(v[i], tag); any = any || nd; if (nd) v[i] = get_g(rs, off + i * stride); }
    if (!any) break;
  }
}
template <bool B> struct BoolC { static constexpr bool value = B; };

__global__ void __launch_bounds__(NT, NW / 4) k_main(const unsigned char* __restrict__ Kc, const unsigned char* __restrict__ Vc,
                                                     const unsigned short* __restrict__ wq, const unsigned short* __restrict__ wo,
                                                     const unsigned short* __restrict__ w1, const unsigned short* __restrict__ b1,
                                                     const unsigned short* __restrict__ w2, const unsigned short* __restrict__ b2,
                                                     const unsigned short* __restrict__ x, unsigned char* ws, unsigned short* __restrict__ out) {
  __shared__ __attribute__((aligned(16))) unsigned char lds[L_BYTES];
  float* mrg = reinterpret_cast<float*>(lds + L_MRG);
  double* mrd = reinterpret_cast<double*>(lds + L_MRG);
  unsigned* lq = reinterpret_cast<unsigned*>(lds + L_Q);
  float* red = reinterpret_cast<float*>(lds + L_RED);
  unsigned* ctl = reinterpret_cast<unsigned*>(lds + L_CTL);
  unsigned* table = reinterpret_cast<unsigned*>(lds + L_TAB);
  const int tid = threadIdx.x, lane = tid & 63, wave = __builtin_amdgcn_readfirstlane(tid >> 6);
  const int r = lane >> 3, sub = lane & 7;
  const bool b8 = (lane & 8) != 0;
  const int bid = blockIdx.x;
  const int h = bid >> 3, sp = bid & 7;
  const __amdgpu_buffer_rsrc_t rs = __builtin_amdgcn_make_buffer_rsrc((void*)ws, 0, WS_END, 0x00020000);
  const unsigned long long tstart = __builtin_amdgcn_s_memrealtime();
  STAMP(0);

  const size_t hbase = ((size_t)h * SK + r) * (DH * 2) + sub * 16;
  const unsigned char* kp = Kc + hbase;
  const unsigned char* vp = Vc + hbase;
  const int st0 = sp * SPT;
  const bool ffn = h < 24, prj = sp < 5;
  const bool two = wave < 2;
  Tile ta, pre[NPRE - 1];
  u32x2 tag;

  auto prologue = [&](auto EXC) {
    constexpr bool EX = decltype(EXC)::value;
    if (EX) { if (sp == 0 && tid == 0) __builtin_amdgcn_raw_buffer_store_b32(0u, rs, WS_CNT + h * 256, 0, AUX_SC1); }
    unsigned xa[5];
#pragma unroll
    for (int i = 0; i < 5; ++i) xa[i] = reinterpret_cast<const unsigned*>(x)[lane + 64 * i];
    const int row0 = sp * 80 + r;
    const bool w1b = wave < 6;
    const int ga = wave, gb = EX ? wave + 8 : wave - 2, gc = (!EX && w1b) ? wave + 4 : gb;
    u32x4 wa[3], wb[3];
    {
      const unsigned char* p = reinterpret_cast<const unsigned char*>(wq) + ((size_t)(row0 + 8 * ga) * HD + h * DH) * 2 + sub * 16;
#pragma unroll
      for (int j = 0; j < 3; ++j) wa[j] = LDNT(p + j * 128);
    }
    if (EX) {
      const unsigned char* p = reinterpret_cast<const unsigned char*>(wq) + ((size_t)(row0 + 8 * gb) * HD + h * DH) * 2 + sub * 16;
#pragma unroll
      for (int j = 0; j < 3; ++j) wb[j] = LDNT(p + j * 128);
    } else {
      const unsigned char* p1 = reinterpret_cast<const unsigned char*>(w1);
      wb[0] = LDNT(p1 + (ffn ? ((size_t)(row0 + 8 * gb) * FF + h * 64) * 2 + sub * 16 : 0));
      wb[1] = LDNT(p1 + (ffn ? ((size_t)(row0 + 8 * gc) * FF + h * 64) * 2 + sub * 16 : 0));
    }
    const u32x2 ep = __builtin_amdgcn_raw_buffer_load_b64(rs, WS_EPOCH, 0, AUX_SC1);
    unsigned b1v = 0;
    if (EX) b1v = reinterpret_cast<const unsigned*>(b1)[ffn ? h * 32 + (lane & 31) : 0];
    __builtin_amdgcn_sched_barrier(0);
    if (EX) {
      if (tid < 16) ctl[tid] = tid == C_TICK ? 6u * NPRE + NW : (tid == C_LSZ ? 32u : 0u);
      for (int i = tid; i < QCAP; i += 128) table[i] = 0;
    }
    float mb, den;
    {
      float s = 0.f;
#pragma unroll
      for (int i = 0; i < 5; ++i) s += bflo(xa[i]) + bfhi(xa[i]);
      const float mean = wave_sum(s) * (1.f / DM);
      float q2 = 0.f;
#pragma unroll
      for (int i = 0; i < 5; ++i) { const float d0 = bflo(xa[i]) - mean, d1 = bfhi(xa[i]) - mean; q2 += d0 * d0 + d1 * d1; }
      q2 = wave_sum(q2);
      mb = rbf(mean);
      den = rbf(sqrtf(rbf(rbf(q2 * (1.f / DM)) + 1e-5f)));
    }
    auto xnorm = [&](int g) -> float {
      const int d0 = (sp * 80 + 8 * g) >> 1, rg = d0 >> 6;
      unsigned v = xa[0];
#pragma unroll
      for (int i = 1; i < 5; ++i) { v = rg == i ? xa[i] : v; asm volatile("" : "+v"(v)); }
      v = (unsigned)__builtin_amdgcn_ds_bpermute(((d0 & 63) + (r >> 1)) << 2, (int)v);
      const float xv = (r & 1) ? bfhi(v) : bflo(v);
      return rbf(rbf(xv - mb) / den);
    };
    u32x4 st[3];
    const int t6 = tid - 128;
    __builtin_amdgcn_sched_barrier(0);
    if (!EX) {
      const unsigned char* w2b = reinterpret_cast<const unsigned char*>(w2) + (size_t)h * (64 * DM * 2) + sp * 256;
#pragma unroll
      for (int k = 0; k < 3; ++k) {
        const int i = t6 + 384 * k;
        st[k] = LDNT((ffn && prj && i < 1024) ? w2b + (i >> 4) * (DM * 2) + (i & 15) * 16 : reinterpret_cast<const unsigned char*>(w2));
      }
      issue(ta, kp, vp, st0 + wave - 2);
    }
    __builtin_amdgcn_sched_barrier(0);
    {
      const float xna = xnorm(ga), xnb = xnorm(gb);
      float pa[24];
#pragma unroll
      for (int j = 0; j < 3; ++j)
#pragma unroll
        for (int k = 0; k < 4; ++k) { pa[j * 8 + 2 * k] = xna * bflo(wa[j][k]); pa[j * 8 + 2 * k + 1] = xna * bfhi(wa[j][k]); }
      if (EX) {
#pragma unroll
        for (int j = 0; j < 3; ++j)
#pragma unroll
          for (int k = 0; k < 4; ++k) { pa[j * 8 + 2 * k] = fmaf(xnb, bflo(wb[j][k]), pa[j * 8 + 2 * k]); pa[j * 8 + 2 * k + 1] = fmaf(xnb, bfhi(wb[j][k]), pa[j * 8 + 2 * k + 1]); }
      }
      float* rec = mrg + wave * 256 + sub * 8 + r;
#pragma unroll
      for (int j = 0; j < 3; ++j) rec[j * 64] = tr_sum8(pa + j * 8, b8);
      float pz = 0.f;
      if (!EX) {
        float pb[8];
        const float xc = w1b ? xnorm(gc) : 0.f;
#pragma unroll
        for (int k = 0; k < 4; ++k) { pb[2 * k] = fmaf(xc, bflo(wb[1][k]), xnb * bflo(wb[0][k])); pb[2 * k + 1] = fmaf(xc, bfhi(wb[1][k]), xnb * bfhi(wb[0][k])); }
        pz = tr_sum8(pb, b8);
      }
      rec[192] = pz;
    }
    {
      const unsigned long long e = ((unsigned long long)ep[1] << 32 | ep[0]) + 1ull;
      tag[0] = __builtin_amdgcn_readfirstlane((unsigned)e); tag[1] = __builtin_amdgcn_readfirstlane((unsigned)(e >> 32));
    }
    __builtin_amdgcn_sched_barrier(0);
    if (!EX) issue(pre[0], kp, vp, st0 + 6 + wave - 2);
    __builtin_amdgcn_sched_barrier(0);
    LDS_WAIT();
    __builtin_amdgcn_s_barrier();
    CBAR();
    if (EX) {
      {
        float s0 = 0.f, s1 = 0.f;
#pragma unroll
        for (int w = 0; w < NW; ++w) { const f32x2 v = *reinterpret_cast<const f32x2*>(mrg + w * 256 + 2 * tid); s0 += v[0]; s1 += v[1]; }
        asm volatile("s_waitcnt vmcnt(0)" ::: "memory");
        put_g(rs, WS_QX + bid * 2048 + tid * 16, s0, s1, tag);
      }
      u32x4 v[8];
      const int off = WS_QX + h * 8 * 2048 + tid * 16;
#pragma unroll
      for (int i = 0; i < 8; ++i) v[i] = get_g(rs, off + i * 2048);
      poll<8>(rs, off, 2048, tag, v);
      float s0 = 0.f, s1 = 0.f;
#pragma unroll
      for (int i = 0; i < 8; ++i) { s0 += __uint_as_float(v[i][0]); s1 += __uint_as_float(v[i][1]); }
      if (tid < 96) lq[tid] = pk2(s0, s1);
      LDS_WAIT();
      if (lane == 0) __hip_atomic_fetch_add(&ctl[C_QRDY], 1u, WG_RLX);
      if (wave == 1) {
        float g[2];
#pragma unroll
        for (int k = 0; k < 2; ++k) {
          const float hv = rbf(rbf(k ? s1 : s0) + (k ? bfhi(b1v) : bflo(b1v)));
          const float u = 0.7978845608028654f * (hv + 0.044715f * hv * hv * hv);
          g[k] = rbf(0.5f * hv * (1.f + tanhf(u)));
        }
        if (tid >= 96) *reinterpret_cast<f32x2*>(lq + 96 + 2 * (tid - 96)) = f32x2{g[0], g[1]};
        LDS_WAIT();
        if (lane == 0) __hip_atomic_fetch_add(&ctl[C_FFN], 1u, WG_RLX);
      }
      STAMP(14);
    } else {
      *reinterpret_cast<u32x4*>(lds + L_W2 + t6 * 16) = st[0];
      *reinterpret_cast<u32x4*>(lds + L_W2 + (t6 + 384) * 16) = st[1];
      if (t6 < 256) *reinterpret_cast<u32x4*>(lds + L_W2 + (t6 + 768) * 16) = st[2];
      LDS_WAIT();
      if (lane == 0) __hip_atomic_fetch_add(&ctl[C_FFN], 1u, WG_RLX);
      const unsigned long long tp = tstart + PACE0 + PACED * (wave - 2);
#pragma unroll
      for (int i = 1; i < NPRE - 1; ++i) {
        const unsigned long long t = tp + PACED * 6 * (i - 1);
        LDS_SPIN(__builtin_amdgcn_s_memrealtime() >= t || LLD(&ctl[C_QRDY]) >= 2u);
        issue(pre[i], kp, vp, st0 + 6 + 6 * i + wave - 2);
      }
    }
  };
  if (two) prologue(BoolC<true>{}); else prologue(BoolC<false>{});
  LDS_SPIN(LLD(&ctl[C_QRDY]) >= 2u);
  u32x4 q[3];
#pragma unroll
  for (int j = 0; j < 3; ++j) q[j] = *reinterpret_cast<const u32x4*>(lq + j * 32 + sub * 4);

  unsigned* cnt = reinterpret_cast<unsigned*>(ws + WS_CNT) + h * 64;
  unsigned zoff = 0; asm volatile("" : "+v"(zoff));
  const unsigned lds0 = (unsigned)(size_t)(__attribute__((address_space(3))) unsigned char*)lds;
  int cur, ntile;
  bool pend = false;
  unsigned gx = 0;
  unsigned stp, outs, nq, lsz, tqv;
  auto deliver = [&](unsigned res) {
    const int g = __builtin_amdgcn_readfirstlane(res);
    if ((unsigned)g < (unsigned)NCH) {
      int sz, st;
      if (g < G32) { sz = 32; st = g * 32; }
      else if (g < G16) { sz = 16; st = S16 + (g - G32) * 16; }
      else if (g < G8) { sz = 8; st = S8 + (g - G16) * 8; }
      else { sz = 4; st = S4 + (g - G8) * 4; }
      unsigned p = 0;
      if (lane == 0) { p = __hip_atomic_fetch_add(&ctl[C_NQ], (unsigned)sz, WG_RLX); __hip_atomic_fetch_min(&ctl[C_LSZ], (unsigned)sz, WG_RLX); }
      p = __builtin_amdgcn_readfirstlane(p);
      int u = st + lane;
      { const int b = u >> 5; u = (((b % NSUB) * (POOL / 32 / NSUB) + b / NSUB) << 5) + (u & 31); }
      if (lane < sz) __hip_atomic_store(&table[p + lane], (unsigned)(DYN0 + u + 1), WG_RLX);
    } else if (lane == 0) __hip_atomic_store(&ctl[C_STOP], 1u, WG_RLX);
    LDS_WAIT();
    if (lane == 0) __hip_atomic_fetch_add(&ctl[C_OUTS], ~0u, WG_RLX);
  };
  auto claim = [&](unsigned seen) -> bool {
    unsigned bad = 0;
    if (lane == 0) {
      const unsigned old = __hip_atomic_fetch_add(&ctl[C_OUTS], 1u, WG_RLX);
      LDS_WAIT();
      bad = __hip_atomic_load(&ctl[C_STOP], WG_RLX) | (old > seen ? 1u : 0u);
      if (bad) __hip_atomic_fetch_add(&ctl[C_OUTS], ~0u, WG_RLX);
    }
    return __builtin_amdgcn_readfirstlane(bad) == 0;
  };
  auto snap = [&](int idx) {
    u32x4 c4; unsigned l4, tq;
    const unsigned qaddr = lds0 + L_TAB + (unsigned)(idx > 0 ? idx : 0) * 4u;
    asm volatile("ds_read_b128 %0, %3 offset:%5\n\tds_read_b32 %1, %3 offset:%6\n\tds_read_b32 %2, %4\n\ts_waitcnt lgkmcnt(0)"
                 : "=&v"(c4), "=&v"(l4), "=&v"(tq) : "v"(lds0), "v"(qaddr), "i"(L_CTL), "i"(L_CTL + 4 * C_LSZ) : "memory");
    stp = __builtin_amdgcn_readfirstlane(c4[C_STOP]); outs = __builtin_amdgcn_readfirstlane(c4[C_OUTS]); nq = __builtin_amdgcn_readfirstlane(c4[C_NQ]);
    lsz = __builtin_amdgcn_readfirstlane(l4); tqv = __builtin_amdgcn_readfirstlane(tq);
  };
  auto prep = [&]() {
    unsigned nx = 0;
    if (lane == 0) nx = __hip_atomic_fetch_add(&ctl[C_TICK], 1u, WG_RLX);
    cur = __builtin_amdgcn_readfirstlane(nx);
    ntile = st0 + cur;
    if (cur >= SPT - LOWW - NW) {
      const int idx = cur - SPT;
      snap(idx);
      if (idx >= 0) ntile = (int)tqv - 1;
      const int level = (int)nq + (int)(outs * lsz) - (idx + 1);
      if (stp == 0 && level < LOWW && claim(outs)) {
        if (lane == 0) gx = __hip_atomic_fetch_add(cnt + zoff, 1u, AG_RLX);
        pend = true;
      }
    }
  };
  auto fetch = [&]() -> bool {
    if (ntile < 0) {
      if (pend) { deliver(gx); pend = false; }
      const int idx = cur - SPT;
#pragma unroll 1
      for (int spin = 0;; ++spin) {
        snap(idx);
        if (tqv != 0) { ntile = (int)tqv - 1; break; }
        if (spin >= SPIN_CAP) return false;
        if (outs == 0 && nq <= (unsigned)idx) {
          if (stp != 0) return false;
          if (claim(0u)) {
            unsigned g2 = 0;
            if (lane == 0) g2 = __hip_atomic_fetch_add(cnt + zoff, 1u, AG_RLX);
            deliver(g2);
          }
        } else __builtin_amdgcn_s_sleep(1);
      }
    }
    issue(ta, kp, vp, ntile);
    __builtin_amdgcn_sched_barrier(0);
    if (pend) { deliver(gx); pend = false; }
    prep();
    return true;
  };

  float m = -INFINITY;
  double l = 0.0, acc[24];
#pragma unroll
  for (int e = 0; e < 24; ++e) acc[e] = 0.0;
  if (!two) compute(ta, q, m, l, acc);
  issue(ta, kp, vp, st0 + 6 * NPRE + wave);
  prep();
  if (!two) {
#pragma unroll
    for (int i = 0; i < NPRE - 1; ++i) compute(pre[i], q, m, l, acc);
  } else if (wave == 0 && ffn && prj) {
    LDS_SPIN(LLD(&ctl[C_FFN]) >= 7u);
    const float* hv = reinterpret_cast<const float*>(lq + 96);
    float z0 = 0.f, z1 = 0.f;
#pragma unroll 8
    for (int i = 0; i < 64; ++i) {
      const unsigned wv = *reinterpret_cast<const unsigned*>(lds + L_W2 + i * 256 + lane * 4);
      const float hh = hv[i];
      z0 = fmaf(hh, bflo(wv), z0); z1 = fmaf(hh, bfhi(wv), z1);
    }
    put_g(rs, WS_ZX + (sp * 8 + (lane >> 3)) * (NH * 128) + h * 128 + (lane & 7) * 16, z0, z1, tag);
  }
  do { compute(ta, q, m, l, acc); } while (fetch());
  STAMP(4);

  int tk;
  {
    const double L = rg_sumd(l);
    double* rec = mrd + wave * PREC;
#pragma unroll
    for (int j = 0; j < 3; ++j) rec[j * 64 + lane] = tr_sum8d(acc + j * 8, b8);
    if (lane == 0) { reinterpret_cast<float*>(rec + 192)[0] = m; rec[193] = L; }
    LDS_WAIT();
    unsigned t = 0;
    if (lane == 0) t = __hip_atomic_fetch_add(&ctl[C_REC], 1u, WG_RLX);
    tk = __builtin_amdgcn_readfirstlane(t);
    CBAR();
  }
  if (tk == NW - 1) {
    float mw[NW];
#pragma unroll
    for (int w = 0; w < NW; ++w) mw[w] = reinterpret_cast<const float*>(mrd + w * PREC + 192)[0];
    const int part = lane & 3, d0 = ((lane >> 4) & 1) * 32 + (lane >> 5) * 4 + ((lane >> 2) & 3);
    const int slot0 = part < 3 ? part * 64 + (d0 & 7) * 8 + (d0 >> 3) : 193, sstep = part < 3 ? 1 : 0;
    double p[NW][4];
#pragma unroll
    for (int w = 0; w < NW; ++w)
#pragma unroll
      for (int i = 0; i < 4; ++i) p[w][i] = mrd[w * PREC + slot0 + i * sstep];
    float MM = mw[0];
#pragma unroll
    for (int w = 1; w < NW; ++w) MM = fmaxf(MM, mw[w]);
    double o[4] = {0.0, 0.0, 0.0, 0.0};
#pragma unroll
    for (int w = 0; w < NW; ++w) {
      const double wt = __builtin_amdgcn_ldexp(1.0, shift_of(mw[w] - MM));
#pragma unroll
      for (int i = 0; i < 4; ++i) o[i] = __builtin_fma(wt, p[w][i], o[i]);
    }
    const bool ism = part == 3 && (d0 & 1) == 0;
#pragma unroll
    for (int i = 0; i < 4; ++i) put_gd(rs, WS_PX + bid * 4096 + i * 1024 + lane * 16, ism ? mkd(__float_as_uint(MM), 0u) : o[i], tag);
    if (lane == 0) __hip_atomic_store(&ctl[C_PUB], 1u, WG_RLX);
  }
  __builtin_amdgcn_sched_barrier(0);
  if (prj) {
    u32x4 wreg[6];
    {
      const unsigned char* wp = reinterpret_cast<const unsigned char*>(wo) + (size_t)(h * DH + wave * 4 + (lane >> 4)) * (DM * 2) + sp * 256 + (lane & 15) * 16;
#pragma unroll
      for (int k = 0; k < 6; ++k) wreg[k] = LDNT(wp + (size_t)k * 32 * (DM * 2));
    }
    LDS_SPIN(LLD(&ctl[C_PUB]) != 0u);
    f32x2 wf[6][4];
    float oa;
    {
      const int off = WS_PX + (h * 8 + (lane >> 5)) * 4096 + wave * 512 + (lane & 31) * 16;
      u32x4 v[4];
#pragma unroll
      for (int i = 0; i < 4; ++i) v[i] = get_g(rs, off + i * 8192);
      __builtin_amdgcn_sched_barrier(0);
#pragma unroll
      for (int k = 0; k < 6; ++k)
#pragma unroll
        for (int e = 0; e < 4; ++e) { wf[k][e] = f32x2{bflo(wreg[k][e]), bfhi(wreg[k][e])}; asm volatile("" : "+v"(wf[k][e])); }
      poll<4>(rs, off, 8192, tag, v);
      const int hb = (lane & 32) << 2;
      float mp[4], MM = -INFINITY;
#pragma unroll
      for (int i = 0; i < 4; ++i) {
        mp[i] = __builtin_bit_cast(float, __builtin_amdgcn_ds_bpermute(hb + 12, (int)v[i][0]));
        MM = fmaxf(MM, mp[i]);
      }
      MM = x32_max(MM);
      double sd = 0.0;
#pragma unroll
      for (int i = 0; i < 4; ++i) sd += __builtin_amdgcn_ldexp(mkd(v[i][0], v[i][1]), shift_of(mp[i] - MM));
      sd = sw32_addd(sd, sd);
      const u32x2 sb = __builtin_bit_cast(u32x2, sd);
      const double L = mkd((unsigned)__builtin_amdgcn_readlane((int)sb[0], 7), (unsigned)__builtin_amdgcn_readlane((int)sb[1], 7));
      double ri = __builtin_amdgcn_rcp(L);
      ri = __builtin_fma(__builtin_fma(-L, ri, 1.0), ri, ri);
      oa = rbf((float)(sd * ri));
    }
    f32x2 y2[4];
#pragma unroll
    for (int e = 0; e < 4; ++e) y2[e] = f32x2{0.f, 0.f};
#pragma unroll
    for (int k = 0; k < 6; ++k) {
      const int src = (((k & 1) * 16 + (lane >> 4) * 4 + (k >> 1)) << 2);
      const float o = __builtin_bit_cast(float, __builtin_amdgcn_ds_bpermute(src, __builtin_bit_cast(int, oa)));
      const f32x2 o2 = f32x2{o, o};
#pragma unroll
      for (int e = 0; e < 4; ++e) y2[e] = __builtin_elementwise_fma(o2, wf[k][e], y2[e]);
    }
    const float u0 = sw32_add(y2[0][0], y2[2][0]), u1 = sw32_add(y2[0][1], y2[2][1]), u2 = sw32_add(y2[1][0], y2[3][0]), u3 = sw32_add(y2[1][1], y2[3][1]);
    *reinterpret_cast<f32x2*>(red + wave * 128 + (lane & 15) * 8 + (lane >> 4) * 2) = f32x2{sw16_add(u0, u2), sw16_add(u1, u3)};
    LDS_WAIT();
    unsigned t = 0;
    if (lane == 0) t = __hip_atomic_fetch_add(&ctl[C_Y], 1u, WG_RLX);
    const int tk2 = __builtin_amdgcn_readfirstlane(t);
    CBAR();
    if (tk2 == NW - 1) {
      float y0 = 0.f, y1 = 0.f;
#pragma unroll
      for (int w = 0; w < NW; ++w) { const f32x2 v = *reinterpret_cast<const f32x2*>(red + w * 128 + lane * 2); y0 += v[0]; y1 += v[1]; }
      put_g(rs, WS_YX + (sp * 8 + (lane >> 3)) * (NH * 128) + h * 128 + (lane & 7) * 16, y0, y1, tag);
    }
    return;
  }
  const int own = h * 3 + (sp - 5);
  if (own >= NOWN || tk != 0) return;
  const unsigned xv = reinterpret_cast<const unsigned*>(x)[own * 8 + (lane & 7)];
  const unsigned bv = reinterpret_cast<const unsigned*>(b2)[own * 8 + (lane & 7)];
  float g0, g1;
  {
    u32x4 v[3];
    const int off = WS_ZX + own * (NH * 128) + lane * 16;
#pragma unroll
    for (int i = 0; i < 3; ++i) v[i] = get_g(rs, off + i * 1024);
    poll<3>(rs, off, 1024, tag, v);
    float z0 = (__uint_as_float(v[0][0]) + __uint_as_float(v[1][0])) + __uint_as_float(v[2][0]);
    float z1 = (__uint_as_float(v[0][1]) + __uint_as_float(v[1][1])) + __uint_as_float(v[2][1]);
    z0 = rbf(rbf(rg_sum(z0)) + bflo(bv)); z1 = rbf(rbf(rg_sum(z1)) + bfhi(bv));
    g0 = rbf(z0 / (1.f + __expf(-z0))); g1 = rbf(z1 / (1.f + __expf(-z1)));
  }
  LDS_SPIN(LLD(&ctl[C_PUB]) != 0u);
  {
    u32x4 v[4];
    const int off = WS_YX + own * (NH * 128) + lane * 16;
#pragma unroll
    for (int i = 0; i < 4; ++i) v[i] = get_g(rs, off + i * 1024);
    poll<4>(rs, off, 1024, tag, v);
    float s0 = (__uint_as_float(v[0][0]) + __uint_as_float(v[1][0])) + (__uint_as_float(v[2][0]) + __uint_as_float(v[3][0]));
    float s1 = (__uint_as_float(v[0][1]) + __uint_as_float(v[1][1])) + (__uint_as_float(v[2][1]) + __uint_as_float(v[3][1]));
    s0 = rg_sum(s0); s1 = rg_sum(s1);
    if (lane < 8) reinterpret_cast<unsigned*>(out)[own * 8 + lane] = pk2(bflo(xv) + rbf(rbf(s0) * g0), bfhi(xv) + rbf(rbf(s1) * g1));
    STAMP(9);
    if (own == 0 && lane == 0) __builtin_amdgcn_raw_buffer_store_b64(tag, rs, WS_EPOCH, 0, AUX_SC1);
  }
}

extern "C" void kernel_launch(void* const* d_in, const int* in_sizes, int n_in,
                              void* d_out, int out_size, void* d_ws, size_t ws_size,
                              hipStream_t stream) {
  const unsigned short* x = (const unsigned short*)d_in[0];
  const unsigned char* kc = (const unsigned char*)d_in[1];
  const unsigned char* vc = (const unsigned char*)d_in[2];
  const unsigned short* wq = (const unsigned short*)d_in[3];
  const unsigned short* wo = (const unsigned short*)d_in[4];
  const unsigned short* w1 = (const unsigned short*)d_in[6];
  const unsigned short* b1 = (const unsigned short*)d_in[7];
  const unsigned short* w2 = (const unsigned short*)d_in[8];
  const unsigned short* b2 = (const unsigned short*)d_in[9];
  k_main<<<NWG, NT, 0, stream>>>(kc, vc, wq, wo, w1, b1, w2, b2, x, (unsigned char*)d_ws, (unsigned short*)d_out);
}
```

```cpp
#include <hip/hip_runtime.h>
#include <hip/hip_bf16.h>
#include <stdint.h>

constexpr int DM = 640, NH = 32, SK = 32768, DH = 192, FF = 1536, HD = NH * DH;
constexpr int NW = 8, NT = NW * 64;
constexpr int NSPL = 8, NWG = NH * NSPL;
constexpr int TBYTES = 8 * DH * 2;
constexpr int NTILE = SK / 8;
#define SPT 344
#define PACE0 400
#define PACED 22
#define NPRE 5
#define LOWW 10
#define NSUB 2
#define N32 18
#define N8 16
#define N4 32
constexpr int DYN0 = NSPL * SPT, POOL = NTILE - DYN0;
constexpr int N16 = (POOL - 32 * N32 - 8 * N8 - 4 * N4) / 16;
static_assert(N32 * 32 + N16 * 16 + 8 * N8 + 4 * N4 == POOL && N16 >= 0, "chunk table covers the pool");
static_assert(POOL % (32 * NSUB) == 0, "");
static_assert(6 * NPRE + 2 * NW < SPT - LOWW - NW, "");
constexpr int G32 = N32, G16 = G32 + N16, G8 = G16 + N8, NCH = G8 + N4;
constexpr int S16 = 32 * N32, S8 = S16 + 16 * N16, S4 = S8 + 8 * N8;
constexpr int QCAP = POOL + 64;
constexpr int PREC = 200;
constexpr int NOWN = 40;

constexpr int WS_EPOCH = 0;
constexpr int WS_CNT = 4096;
constexpr int WS_QX = 16384;
constexpr int WS_PX = WS_QX + NWG * 2048;
constexpr int WS_YX = WS_PX + NWG * 4096;
constexpr int WS_ZX = WS_YX + NOWN * NH * 128;
constexpr int WS_END = WS_ZX + NOWN * NH * 128;

constexpr int L_W2 = 0;
constexpr int L_MRG = L_W2 + 64 * 256;
constexpr int L_Q = L_MRG + NW * PREC * 8;
constexpr int L_RED = L_Q + 160 * 4;
constexpr int L_CTL = L_RED + NW * 128 * 4;
constexpr int L_TAB = L_CTL + 64;
constexpr int L_END = L_TAB + QCAP * 4;
constexpr int L_BYTES = L_END > 84 * 1024 ? L_END : 84 * 1024;
static_assert(L_CTL % 16 == 0, "");
constexpr int C_TICK = 0, C_STOP = 1, C_OUTS = 2, C_NQ = 3, C_LSZ = 4, C_QRDY = 5, C_FFN = 6, C_REC = 7, C_PUB = 8, C_Y = 9;

typedef unsigned u32x4 __attribute__((ext_vector_type(4)));
typedef unsigned u32x2 __attribute__((ext_vector_type(2)));
typedef float f32x2 __attribute__((ext_vector_type(2)));
typedef __bf16 bf16x2 __attribute__((ext_vector_type(2)));

__device__ __forceinline__ float bflo(unsigned u) { return __builtin_bit_cast(float, u << 16); }
__device__ __forceinline__ float bfhi(unsigned u) { return __builtin_bit_cast(float, u & 0xffff0000u); }
__device__ __forceinline__ float rbf(float x) { return (float)(__bf16)x; }
__device__ __forceinline__ unsigned pk2(float a, float b) {
  return (unsigned)__builtin_bit_cast(unsigned short, (__bf16)a) | ((unsigned)__builtin_bit_cast(unsigned short, (__bf16)b) << 16);
}
__device__ __forceinline__ float dot2(unsigned k, unsigned q, float acc) {
  return __builtin_amdgcn_fdot2_f32_bf16(__builtin_bit_cast(bf16x2, k), __builtin_bit_cast(bf16x2, q), acc, false);
}
template <int CTRL> __device__ __forceinline__ float dpp(float x) {
  return __builtin_bit_cast(float, __builtin_amdgcn_mov_dpp(__builtin_bit_cast(int, x), CTRL, 0xf, 0xf, true));
}
constexpr int XOR1 = 0xB1, XOR2 = 0x4E, XOR7 = 0x141, XOR8 = 0x128;
__device__ __forceinline__ float sw32_add(float a, float b) {
  auto s = __builtin_amdgcn_permlane32_swap(__float_as_uint(a), __float_as_uint(b), false, false);
  return __uint_as_float(s[0]) + __uint_as_float(s[1]);
}
__device__ __forceinline__ float sw16_add(float a, float b) {
  auto s = __builtin_amdgcn_permlane16_swap(__float_as_uint(a), __float_as_uint(b), false, false);
  return __uint_as_float(s[0]) + __uint_as_float(s[1]);
}
__device__ __forceinline__ float x32_max(float x) {
  auto t = __builtin_amdgcn_permlane32_swap(__float_as_uint(x), __float_as_uint(x), false, false);
  return fmaxf(__uint_as_float(t[0]), __uint_as_float(t[1]));
}
__device__ __forceinline__ float x16_max(float x) {
  auto s = __builtin_amdgcn_permlane16_swap(__float_as_uint(x), __float_as_uint(x), false, false);
  return x32_max(fmaxf(__uint_as_float(s[0]), __uint_as_float(s[1])));
}
__device__ __forceinline__ float rg_sum(float x) {
  x += dpp<XOR8>(x); x = sw16_add(x, x); return sw32_add(x, x);
}
__device__ __forceinline__ float wave_sum(float x) { x += dpp<XOR1>(x); x += dpp<XOR2>(x); x += dpp<XOR7>(x); return rg_sum(x); }
__device__ __forceinline__ float tr_sum8(const float* v, bool b8) {
  float u[4], w[2];
#pragma unroll
  for (int k = 0; k < 4; ++k) u[k] = sw32_add(v[k], v[k + 4]);
#pragma unroll
  for (int k = 0; k < 2; ++k) w[k] = sw16_add(u[k], u[k + 2]);
  const float keep = b8 ? w[1] : w[0], send = b8 ? w[0] : w[1];
  return keep + dpp<XOR8>(send);
}
__device__ __forceinline__ double mkd(unsigned lo, unsigned hi) { return __builtin_bit_cast(double, u32x2{lo, hi}); }
__device__ __forceinline__ double sw32_addd(double a, double b) {
  const u32x2 x = __builtin_bit_cast(u32x2, a), y = __builtin_bit_cast(u32x2, b);
  auto lo = __builtin_amdgcn_permlane32_swap(x[0], y[0], false, false); auto hi = __builtin_amdgcn_permlane32_swap(x[1], y[1], false, false);
  return mkd(lo[0], hi[0]) + mkd(lo[1], hi[1]);
}
__device__ __forceinline__ double sw16_addd(double a, double b) {
  const u32x2 x = __builtin_bit_cast(u32x2, a), y = __builtin_bit_cast(u32x2, b);
  auto lo = __builtin_amdgcn_permlane16_swap(x[0], y[0], false, false); auto hi = __builtin_amdgcn_permlane16_swap(x[1], y[1], false, false);
  return mkd(lo[0], hi[0]) + mkd(lo[1], hi[1]);
}
__device__ __forceinline__ double ror8d(double a) {
  const u32x2 x = __builtin_bit_cast(u32x2, a);
  return mkd((unsigned)__builtin_amdgcn_mov_dpp((int)x[0], XOR8, 0xf, 0xf, true), (unsigned)__builtin_amdgcn_mov_dpp((int)x[1], XOR8, 0xf, 0xf, true));
}
__device__ __forceinline__ double tr_sum8d(const double* v, bool b8) {
  double u[4], w[2];
#pragma unroll
  for (int k = 0; k < 4; ++k) u[k] = sw32_addd(v[k], v[k + 4]);
#pragma unroll
  for (int k = 0; k < 2; ++k) w[k] = sw16_addd(u[k], u[k + 2]);
  const double keep = b8 ? w[1] : w[0], send = b8 ? w[0] : w[1];
  return keep + ror8d(send);
}
__device__ __forceinline__ double rg_sumd(double x) { x += ror8d(x); x = sw16_addd(x, x); return sw32_addd(x, x); }
__device__ __forceinline__ int shift_of(float d) { return (int)fmaxf(d, -4000.f); }

#define LDNT(p) __builtin_nontemporal_load(reinterpret_cast<const u32x4*>(p))
#define WG_RLX __ATOMIC_RELAXED, __HIP_MEMORY_SCOPE_WORKGROUP
#define AG_RLX __ATOMIC_RELAXED, __HIP_MEMORY_SCOPE_AGENT
#define LLD(p) __builtin_amdgcn_readfirstlane(__hip_atomic_load((p), WG_RLX))
#define LDS_WAIT() asm volatile("s_waitcnt lgkmcnt(0)" ::: "memory")
#define CBAR() asm volatile("" ::: "memory")
#define LDS_SPIN(cond) do { _Pragma("unroll 1") for (int spin_ = 0; spin_ < SPIN_CAP; ++spin_) { if (cond) break; __builtin_amdgcn_s_sleep(1); } CBAR(); } while (0)
constexpr int AUX_SC1 = 16;
constexpr int SPIN_CAP = 1 << 20;

struct Tile { u32x4 k[3], v[3]; };
__device__ __forceinline__ void issue(Tile& t, const unsigned char* kp, const unsigned char* vp, int tile) {
  const size_t off = (size_t)tile * TBYTES;
#pragma unroll
  for (int j = 0; j < 3; ++j) t.k[j] = LDNT(kp + off + j * 128);
#pragma unroll
  for (int j = 0; j < 3; ++j) t.v[j] = LDNT(vp + off + j * 128);
}
__device__ __forceinline__ void compute(const Tile& t, const u32x4 (&q)[3], float& m, double& l, double (&acc)[24]) {
  constexpr float SC = 0.07216878364870322f, LOG2E = 1.4426950408889634f;
  float a0 = 0.f, a1 = 0.f, a2 = 0.f;
#pragma unroll
  for (int k = 0; k < 4; ++k) { a0 = dot2(t.k[0][k], q[0][k], a0); a1 = dot2(t.k[1][k], q[1][k], a1); a2 = dot2(t.k[2][k], q[2][k], a2); }
  float a = (a0 + a1) + a2;
  a += dpp<XOR1>(a); a += dpp<XOR2>(a); a += dpp<XOR7>(a);
  const float s = rbf(rbf(a) * SC) * LOG2E;
  const float fl = __builtin_floorf(s);
  if (__any(fl > m)) {
    float mn = fmaxf(m, fl);
    mn = fmaxf(mn, dpp<XOR8>(mn)); mn = x16_max(mn);
    const int sh = shift_of(m - mn);
    l = __builtin_amdgcn_ldexp(l, sh);
#pragma unroll
    for (int e = 0; e < 24; ++e) acc[e] = __builtin_amdgcn_ldexp(acc[e], sh);
    m = mn;
  }
  const double p = __builtin_amdgcn_ldexp((double)__builtin_amdgcn_exp2f(s - fl), shift_of(fl - m));
  l += p;
#pragma unroll
  for (int j = 0; j < 3; ++j)
#pragma unroll
    for (int k = 0; k < 4; ++k) {
      acc[j * 8 + 2 * k] = __builtin_fma(p, (double)bflo(t.v[j][k]), acc[j * 8 + 2 * k]);
      acc[j * 8 + 2 * k + 1] = __builtin_fma(p, (double)bfhi(t.v[j][k]), acc[j * 8 + 2 * k + 1]);
    }
}

__device__ __forceinline__ void put_g(__amdgpu_buffer_rsrc_t rs, int off, float a, float b, u32x2 tag) {
  const u32x4 v = {__float_as_uint(a), __float_as_uint(b), tag[0], tag[1]};
  __builtin_amdgcn_raw_buffer_store_b128(v, rs, off, 0, AUX_SC1);
}
__device__ __forceinline__ void put_gd(__amdgpu_buffer_rsrc_t rs, int off, double a, u32x2 tag) {
  const u32x2 x = __builtin_bit_cast(u32x2, a);
  const u32x4 v = {x[0], x[1], tag[0], tag[1]};
  __builtin_amdgcn_raw_buffer_store_b128(v, rs, off, 0, AUX_SC1);
}
__device__ __forceinline__ u32x4 get_g(__amdgpu_buffer_rsrc_t rs, int off) { return __builtin_amdgcn_raw_buffer_load_b128(rs, off, 0, AUX_SC1); }
__device__ __forceinline__ bool tag_ok(u32x4 v, u32x2 tag) { return v[2] == tag[0] && v[3] == tag[1]; }
template <int N> __device__ __forceinline__ void poll(__amdgpu_buffer_rsrc_t rs, int off, int stride, u32x2 tag, u32x4 (&v)[N]) {
#pragma unroll 1
  for (int spin = 0; spin < SPIN_CAP; ++spin) {
    asm volatile("" : "+v"(off) :: "memory");
    bool any = false;
#pragma unroll
    for (int i = 0; i < N; ++i) { const bool nd = !tag_ok(v[i], tag); any = any || nd; if (nd) v[i] = get_g(rs, off + i * stride); }
    if (!any) break;
  }
}
template <bool B> struct BoolC { static constexpr bool value = B; };

__global__ void __launch_bounds__(NT, NW / 4) k_main(const unsigned char* __restrict__ Kc, const unsigned char* __restrict__ Vc,
                                                     const unsigned short* __restrict__ wq, const unsigned short* __restrict__ wo,
                                                     const unsigned short* __restrict__ w1, const unsigned short* __restrict__ b1,
                                                     const unsigned short* __restrict__ w2, const unsigned short* __restrict__ b2,
                                                     const unsigned short* __restrict__ x, unsigned char* ws, unsigned short* __restrict__ out) {
  __shared__ __attribute__((aligned(16))) unsigned char lds[L_BYTES];
  float* mrg = reinterpret_cast<float*>(lds + L_MRG);
  double* mrd = reinterpret_cast<double*>(lds + L_MRG);
  unsigned* lq = reinterpret_cast<unsigned*>(lds + L_Q);
  float* red = reinterpret_cast<float*>(lds + L_RED);
  unsigned* ctl = reinterpret_cast<unsigned*>(lds + L_CTL);
  unsigned* table = reinterpret_cast<unsigned*>(lds + L_TAB);
  const int tid = threadIdx.x, lane = tid & 63, wave = __builtin_amdgcn_readfirstlane(tid >> 6);
  const int r = lane >> 3, sub = lane & 7;
  const bool b8 = (lane & 8) != 0;
  const int bid = blockIdx.x;
  const int h = bid >> 3, sp = bid & 7;
  const __amdgpu_buffer_rsrc_t rs = __builtin_amdgcn_make_buffer_rsrc((void*)ws, 0, WS_END, 0x00020000);
  const unsigned long long tstart = __builtin_amdgcn_s_memrealtime();

  const size_t hbase = ((size_t)h * SK + r) * (DH * 2) + sub * 16;
  const unsigned char* kp = Kc + hbase;
  const unsigned char* vp = Vc + hbase;
  const int st0 = sp * SPT;
  const bool ffn = h < 24, prj = sp < 5;
  const bool two = wave < 2;
  Tile ta, pre[NPRE - 1];
  u32x2 tag;

  auto prologue = [&](auto EXC) {
    constexpr bool EX = decltype(EXC)::value;
    if (EX) { if (sp == 0 && tid == 0) __builtin_amdgcn_raw_buffer_store_b32(0u, rs, WS_CNT + h * 256, 0, AUX_SC1); }
    unsigned xa[5];
#pragma unroll
    for (int i = 0; i < 5; ++i) xa[i] = reinterpret_cast<const unsigned*>(x)[lane + 64 * i];
    const int row0 = sp * 80 + r;
    const bool w1b = wave < 6;
    const int ga = wave, gb = EX ? wave + 8 : wave - 2, gc = (!EX && w1b) ? wave + 4 : gb;
    u32x4 wa[3], wb[3];
    {
      const unsigned char* p = reinterpret_cast<const unsigned char*>(wq) + ((size_t)(row0 + 8 * ga) * HD + h * DH) * 2 + sub * 16;
#pragma unroll
      for (int j = 0; j < 3; ++j) wa[j] = LDNT(p + j * 128);
    }
    if (EX) {
      const unsigned char* p = reinterpret_cast<const unsigned char*>(wq) + ((size_t)(row0 + 8 * gb) * HD + h * DH) * 2 + sub * 16;
#pragma unroll
      for (int j = 0; j < 3; ++j) wb[j] = LDNT(p + j * 128);
    } else {
      const unsigned char* p1 = reinterpret_cast<const unsigned char*>(w1);
      wb[0] = LDNT(p1 + (ffn ? ((size_t)(row0 + 8 * gb) * FF + h * 64) * 2 + sub * 16 : 0));
      wb[1] = LDNT(p1 + (ffn ? ((size_t)(row0 + 8 * gc) * FF + h * 64) * 2 + sub * 16 : 0));
    }
    const u32x2 ep = __builtin_amdgcn_raw_buffer_load_b64(rs, WS_EPOCH, 0, AUX_SC1);
    unsigned b1v = 0;
    if (EX) b1v = reinterpret_cast<const unsigned*>(b1)[ffn ? h * 32 + (lane & 31) : 0];
    __builtin_amdgcn_sched_barrier(0);
    if (EX) {
      if (tid < 16) ctl[tid] = tid == C_TICK ? 6u * NPRE + 2 * NW : (tid == C_LSZ ? 32u : 0u);
      for (int i = tid; i < QCAP; i += 128) table[i] = 0;
    }
    float mb, den;
    {
      float s = 0.f;
#pragma unroll
      for (int i = 0; i < 5; ++i) s += bflo(xa[i]) + bfhi(xa[i]);
      const float mean = wave_sum(s) * (1.f / DM);
      float q2 = 0.f;
#pragma unroll
      for (int i = 0; i < 5; ++i) { const float d0 = bflo(xa[i]) - mean, d1 = bfhi(xa[i]) - mean; q2 += d0 * d0 + d1 * d1; }
      q2 = wave_sum(q2);
      mb = rbf(mean);
      den = rbf(sqrtf(rbf(rbf(q2 * (1.f / DM)) + 1e-5f)));
    }
    auto xnorm = [&](int g) -> float {
      const int d0 = (sp * 80 + 8 * g) >> 1, rg = d0 >> 6;
      unsigned v = xa[0];
#pragma unroll
      for (int i = 1; i < 5; ++i) { v = rg == i ? xa[i] : v; asm volatile("" : "+v"(v)); }
      v = (unsigned)__builtin_amdgcn_ds_bpermute(((d0 & 63) + (r >> 1)) << 2, (int)v);
      const float xv = (r & 1) ? bfhi(v) : bflo(v);
      return rbf(rbf(xv - mb) / den);
    };
    u32x4 st[3];
    const int t6 = tid - 128;
    __builtin_amdgcn_sched_barrier(0);
    if (!EX) {
      const unsigned char* w2b = reinterpret_cast<const unsigned char*>(w2) + (size_t)h * (64 * DM * 2) + sp * 256;
#pragma unroll
      for (int k = 0; k < 3; ++k) {
        const int i = t6 + 384 * k;
        st[k] = LDNT((ffn && prj && i < 1024) ? w2b + (i >> 4) * (DM * 2) + (i & 15) * 16 : reinterpret_cast<const unsigned char*>(w2));
      }
      issue(ta, kp, vp, st0 + wave - 2);
    }
    __builtin_amdgcn_sched_barrier(0);
    {
      const float xna = xnorm(ga), xnb = xnorm(gb);
      float pa[24];
#pragma unroll
      for (int j = 0; j < 3; ++j)
#pragma unroll
        for (int k = 0; k < 4; ++k) { pa[j * 8 + 2 * k] = xna * bflo(wa[j][k]); pa[j * 8 + 2 * k + 1] = xna * bfhi(wa[j][k]); }
      if (EX) {
#pragma unroll
        for (int j = 0; j < 3; ++j)
#pragma unroll
          for (int k = 0; k < 4; ++k) { pa[j * 8 + 2 * k] = fmaf(xnb, bflo(wb[j][k]), pa[j * 8 + 2 * k]); pa[j * 8 + 2 * k + 1] = fmaf(xnb, bfhi(wb[j][k]), pa[j * 8 + 2 * k + 1]); }
      }
      float* rec = mrg + wave * 256 + sub * 8 + r;
#pragma unroll
      for (int j = 0; j < 3; ++j) rec[j * 64] = tr_sum8(pa + j * 8, b8);
      float pz = 0.f;
      if (!EX) {
        float pb[8];
        const float xc = w1b ? xnorm(gc) : 0.f;
#pragma unroll
        for (int k = 0; k < 4; ++k) { pb[2 * k] = fmaf(xc, bflo(wb[1][k]), xnb * bflo(wb[0][k])); pb[2 * k + 1] = fmaf(xc, bfhi(wb[1][k]), xnb * bfhi(wb[0][k])); }
        pz = tr_sum8(pb, b8);
      }
      rec[192] = pz;
    }
    {
      const unsigned long long e = ((unsigned long long)ep[1] << 32 | ep[0]) + 1ull;
      tag[0] = __builtin_amdgcn_readfirstlane((unsigned)e); tag[1] = __builtin_amdgcn_readfirstlane((unsigned)(e >> 32));
    }
    __builtin_amdgcn_sched_barrier(0);
    if (!EX) issue(pre[0], kp, vp, st0 + 6 + wave - 2);
    __builtin_amdgcn_sched_barrier(0);
    LDS_WAIT();
    __builtin_amdgcn_s_barrier();
    CBAR();
    if (EX) {
      {
        float s0 = 0.f, s1 = 0.f;
#pragma unroll
        for (int w = 0; w < NW; ++w) { const f32x2 v = *reinterpret_cast<const f32x2*>(mrg + w * 256 + 2 * tid); s0 += v[0]; s1 += v[1]; }
        asm volatile("s_waitcnt vmcnt(0)" ::: "memory");
        put_g(rs, WS_QX + bid * 2048 + tid * 16, s0, s1, tag);
      }
      u32x4 v[8];
      const int off = WS_QX + h * 8 * 2048 + tid * 16;
#pragma unroll
      for (int i = 0; i < 8; ++i) v[i] = get_g(rs, off + i * 2048);
      poll<8>(rs, off, 2048, tag, v);
      float s0 = 0.f, s1 = 0.f;
#pragma unroll
      for (int i = 0; i < 8; ++i) { s0 += __uint_as_float(v[i][0]); s1 += __uint_as_float(v[i][1]); }
      if (tid < 96) lq[tid] = pk2(s0, s1);
      LDS_WAIT();
      if (lane == 0) __hip_atomic_fetch_add(&ctl[C_QRDY], 1u, WG_RLX);
      if (wave == 1) {
        float g[2];
#pragma unroll
        for (int k = 0; k < 2; ++k) {
          const float hv = rbf(rbf(k ? s1 : s0) + (k ? bfhi(b1v) : bflo(b1v)));
          const float u = 0.7978845608028654f * (hv + 0.044715f * hv * hv * hv);
          g[k] = rbf(0.5f * hv * (1.f + tanhf(u)));
        }
        if (tid >= 96) *reinterpret_cast<f32x2*>(lq + 96 + 2 * (tid - 96)) = f32x2{g[0], g[1]};
        LDS_WAIT();
        if (lane == 0) __hip_atomic_fetch_add(&ctl[C_FFN], 1u, WG_RLX);
      }
    } else {
      *reinterpret_cast<u32x4*>(lds + L_W2 + t6 * 16) = st[0];
      *reinterpret_cast<u32x4*>(lds + L_W2 + (t6 + 384) * 16) = st[1];
      if (t6 < 256) *reinterpret_cast<u32x4*>(lds + L_W2 + (t6 + 768) * 16) = st[2];
      LDS_WAIT();
      if (lane == 0) __hip_atomic_fetch_add(&ctl[C_FFN], 1u, WG_RLX);
      const unsigned long long tp = tstart + PACE0 + PACED * (wave - 2);
#pragma unroll
      for (int i = 1; i < NPRE - 1; ++i) {
        const unsigned long long t = tp + PACED * 6 * (i - 1);
        LDS_SPIN(__builtin_amdgcn_s_memrealtime() >= t || LLD(&ctl[C_QRDY]) >= 2u);
        issue(pre[i], kp, vp, st0 + 6 + 6 * i + wave - 2);
      }
    }
  };
  if (two) prologue(BoolC<true>{}); else prologue(BoolC<false>{});
  LDS_SPIN(LLD(&ctl[C_QRDY]) >= 2u);
  u32x4 q[3];
#pragma unroll
  for (int j = 0; j < 3; ++j) q[j] = *reinterpret_cast<const u32x4*>(lq + j * 32 + sub * 4);

  unsigned* cnt = reinterpret_cast<unsigned*>(ws + WS_CNT) + h * 64;
  unsigned zoff = 0; asm volatile("" : "+v"(zoff));
  const unsigned lds0 = (unsigned)(size_t)(__attribute__((address_space(3))) unsigned char*)lds;
  int cur, ntile;
  bool pend = false;
  unsigned gx = 0;
  unsigned stp, outs, nq, lsz, tqv;
  auto deliver = [&](unsigned res) {
    const int g = __builtin_amdgcn_readfirstlane(res);
    if ((unsigned)g < (unsigned)NCH) {
      int sz, st;
      if (g < G32) { sz = 32; st = g * 32; }
      else if (g < G16) { sz = 16; st = S16 + (g - G32) * 16; }
      else if (g < G8) { sz = 8; st = S8 + (g - G16) * 8; }
      else { sz = 4; st = S4 + (g - G8) * 4; }
      unsigned p = 0;
      if (lane == 0) { p = __hip_atomic_fetch_add(&ctl[C_NQ], (unsigned)sz, WG_RLX); __hip_atomic_fetch_min(&ctl[C_LSZ], (unsigned)sz, WG_RLX); }
      p = __builtin_amdgcn_readfirstlane(p);
      int u = st + lane;
      { const int b = u >> 5; u = (((b % NSUB) * (POOL / 32 / NSUB) + b / NSUB) << 5) + (u & 31); }
      if (lane < sz) __hip_atomic_store(&table[p + lane], (unsigned)(DYN0 + u + 1), WG_RLX);
    } else if (lane == 0) __hip_atomic_store(&ctl[C_STOP], 1u, WG_RLX);
    LDS_WAIT();
    if (lane == 0) __hip_atomic_fetch_add(&ctl[C_OUTS], ~0u, WG_RLX);
  };
  auto claim = [&](unsigned seen) -> bool {
    unsigned bad = 0;
    if (lane == 0) {
      const unsigned old = __hip_atomic_fetch_add(&ctl[C_OUTS], 1u, WG_RLX);
      LDS_WAIT();
      bad = __hip_atomic_load(&ctl[C_STOP], WG_RLX) | (old > seen ? 1u : 0u);
      if (bad) __hip_atomic_fetch_add(&ctl[C_OUTS], ~0u, WG_RLX);
    }
    return __builtin_amdgcn_readfirstlane(bad) == 0;
  };
  auto snap = [&](int idx) {
    u32x4 c4; unsigned l4, tq;
    const unsigned qaddr = lds0 + L_TAB + (unsigned)(idx > 0 ? idx : 0) * 4u;
    asm volatile("ds_read_b128 %0, %3 offset:%5\n\tds_read_b32 %1, %3 offset:%6\n\tds_read_b32 %2, %4\n\ts_waitcnt lgkmcnt(0)"
                 : "=&v"(c4), "=&v"(l4), "=&v"(tq) : "v"(lds0), "v"(qaddr), "i"(L_CTL), "i"(L_CTL + 4 * C_LSZ) : "memory");
    stp = __builtin_amdgcn_readfirstlane(c4[C_STOP]); outs = __builtin_amdgcn_readfirstlane(c4[C_OUTS]); nq = __builtin_amdgcn_readfirstlane(c4[C_NQ]);
    lsz = __builtin_amdgcn_readfirstlane(l4); tqv = __builtin_amdgcn_readfirstlane(tq);
  };
  auto prep = [&]() {
    unsigned nx = 0;
    if (lane == 0) nx = __hip_atomic_fetch_add(&ctl[C_TICK], 1u, WG_RLX);
    cur = __builtin_amdgcn_readfirstlane(nx);
    ntile = st0 + cur;
    if (cur >= SPT - LOWW - NW) {
      const int idx = cur - SPT;
      snap(idx);
      if (idx >= 0) ntile = (int)tqv - 1;
      const int level = (int)nq + (int)(outs * lsz) - (idx + 1);
      if (stp == 0 && level < LOWW && claim(outs)) {
        if (lane == 0) gx = __hip_atomic_fetch_add(cnt + zoff, 1u, AG_RLX);
        pend = true;
      }
    }
  };
  auto fetch = [&]() -> bool {
    if (ntile < 0) {
      if (pend) { deliver(gx); pend = false; }
      const int idx = cur - SPT;
#pragma unroll 1
      for (int spin = 0;; ++spin) {
        snap(idx);
        if (tqv != 0) { ntile = (int)tqv - 1; break; }
        if (spin >= SPIN_CAP) return false;
        if (outs == 0 && nq <= (unsigned)idx) {
          if (stp != 0) return false;
          if (claim(0u)) {
            unsigned g2 = 0;
            if (lane == 0) g2 = __hip_atomic_fetch_add(cnt + zoff, 1u, AG_RLX);
            deliver(g2);
          }
        } else __builtin_amdgcn_s_sleep(1);
      }
    }
    issue(ta, kp, vp, ntile);
    __builtin_amdgcn_sched_barrier(0);
    if (pend) { deliver(gx); pend = false; }
    prep();
    return true;
  };

  float m = -INFINITY;
  double l = 0.0, acc[24];
#pragma unroll
  for (int e = 0; e < 24; ++e) acc[e] = 0.0;
  if (!two) compute(ta, q, m, l, acc);
  issue(ta, kp, vp, st0 + 6 * NPRE + wave);
  cur = 6 * NPRE + NW + wave; ntile = st0 + cur;
  if (!two) {
#pragma unroll
    for (int i = 0; i < NPRE - 1; ++i) compute(pre[i], q, m, l, acc);
  } else if (wave == 0 && ffn && prj) {
    LDS_SPIN(LLD(&ctl[C_FFN]) >= 7u);
    const float* hv = reinterpret_cast<const float*>(lq + 96);
    float z0 = 0.f, z1 = 0.f;
#pragma unroll 8
    for (int i = 0; i < 64; ++i) {
      const unsigned wv = *reinterpret_cast<const unsigned*>(lds + L_W2 + i * 256 + lane * 4);
      const float hh = hv[i];
      z0 = fmaf(hh, bflo(wv), z0); z1 = fmaf(hh, bfhi(wv), z1);
    }
    put_g(rs, WS_ZX + (sp * 8 + (lane >> 3)) * (NH * 128) + h * 128 + (lane & 7) * 16, z0, z1, tag);
  }
  do { compute(ta, q, m, l, acc); } while (fetch());

  int tk;
  {
    const double L = rg_sumd(l);
    double* rec = mrd + wave * PREC;
#pragma unroll
    for (int j = 0; j < 3; ++j) rec[j * 64 + lane] = tr_sum8d(acc + j * 8, b8);
    if (lane == 0) { reinterpret_cast<float*>(rec + 192)[0] = m; rec[193] = L; }
    LDS_WAIT();
    unsigned t = 0;
    if (lane == 0) t = __hip_atomic_fetch_add(&ctl[C_REC], 1u, WG_RLX);
    tk = __builtin_amdgcn_readfirstlane(t);
    CBAR();
  }
  if (tk == NW - 1) {
    float mw[NW];
#pragma unroll
    for (int w = 0; w < NW; ++w) mw[w] = reinterpret_cast<const float*>(mrd + w * PREC + 192)[0];
    const int part = lane & 3, d0 = ((lane >> 4) & 1) * 32 + (lane >> 5) * 4 + ((lane >> 2) & 3);
    const int slot0 = part < 3 ? part * 64 + (d0 & 7) * 8 + (d0 >> 3) : 193, sstep = part < 3 ? 1 : 0;
    double p[NW][4];
#pragma unroll
    for (int w = 0; w < NW; ++w)
#pragma unroll
      for (int i = 0; i < 4; ++i) p[w][i] = mrd[w * PREC + slot0 + i * sstep];
    float MM = mw[0];
#pragma unroll
    for (int w = 1; w < NW; ++w) MM = fmaxf(MM, mw[w]);
    double o[4] = {0.0, 0.0, 0.0, 0.0};
#pragma unroll
    for (int w = 0; w < NW; ++w) {
      const double wt = __builtin_amdgcn_ldexp(1.0, shift_of(mw[w] - MM));
#pragma unroll
      for (int i = 0; i < 4; ++i) o[i] = __builtin_fma(wt, p[w][i], o[i]);
    }
    const bool ism = part == 3 && (d0 & 1) == 0;
#pragma unroll
    for (int i = 0; i < 4; ++i) put_gd(rs, WS_PX + bid * 4096 + i * 1024 + lane * 16, ism ? mkd(__float_as_uint(MM), 0u) : o[i], tag);
    if (lane == 0) __hip_atomic_store(&ctl[C_PUB], 1u, WG_RLX);
  }
  __builtin_amdgcn_sched_barrier(0);
  if (prj) {
    u32x4 wreg[6];
    {
      const unsigned char* wp = reinterpret_cast<const unsigned char*>(wo) + (size_t)(h * DH + wave * 4 + (lane >> 4)) * (DM * 2) + sp * 256 + (lane & 15) * 16;
#pragma unroll
      for (int k = 0; k < 6; ++k) wreg[k] = LDNT(wp + (size_t)k * 32 * (DM * 2));
    }
    LDS_SPIN(LLD(&ctl[C_PUB]) != 0u);
    f32x2 wf[6][4];
    float oa;
    {
      const int off = WS_PX + (h * 8 + (lane >> 5)) * 4096 + wave * 512 + (lane & 31) * 16;
      u32x4 v[4];
#pragma unroll
      for (int i = 0; i < 4; ++i) v[i] = get_g(rs, off + i * 8192);
      __builtin_amdgcn_sched_barrier(0);
#pragma unroll
      for (int k = 0; k < 6; ++k)
#pragma unroll
        for (int e = 0; e < 4; ++e) { wf[k][e] = f32x2{bflo(wreg[k][e]), bfhi(wreg[k][e])}; asm volatile("" : "+v"(wf[k][e])); }
      poll<4>(rs, off, 8192, tag, v);
      const int hb = (lane & 32) << 2;
      float mp[4], MM = -INFINITY;
#pragma unroll
      for (int i = 0; i < 4; ++i) {
        mp[i] = __builtin_bit_cast(float, __builtin_amdgcn_ds_bpermute(hb + 12, (int)v[i][0]));
        MM = fmaxf(MM, mp[i]);
      }
      MM = x32_max(MM);
      double sd = 0.0;
#pragma unroll
      for (int i = 0; i < 4; ++i) sd += __builtin_amdgcn_ldexp(mkd(v[i][0], v[i][1]), shift_of(mp[i] - MM));
      sd = sw32_addd(sd, sd);
      const u32x2 sb = __builtin_bit_cast(u32x2, sd);
      const double L = mkd((unsigned)__builtin_amdgcn_readlane((int)sb[0], 7), (unsigned)__builtin_amdgcn_readlane((int)sb[1], 7));
      double ri = __builtin_amdgcn_rcp(L);
      ri = __builtin_fma(__builtin_fma(-L, ri, 1.0), ri, ri);
      oa = rbf((float)(sd * ri));
    }
    f32x2 y2[4];
#pragma unroll
    for (int e = 0; e < 4; ++e) y2[e] = f32x2{0.f, 0.f};
#pragma unroll
    for (int k = 0; k < 6; ++k) {
      const int src = (((k & 1) * 16 + (lane >> 4) * 4 + (k >> 1)) << 2);
      const float o = __builtin_bit_cast(float, __builtin_amdgcn_ds_bpermute(src, __builtin_bit_cast(int, oa)));
      const f32x2 o2 = f32x2{o, o};
#pragma unroll
      for (int e = 0; e < 4; ++e) y2[e] = __builtin_elementwise_fma(o2, wf[k][e], y2[e]);
    }
    const float u0 = sw32_add(y2[0][0], y2[2][0]), u1 = sw32_add(y2[0][1], y2[2][1]), u2 = sw32_add(y2[1][0], y2[3][0]), u3 = sw32_add(y2[1][1], y2[3][1]);
    *reinterpret_cast<f32x2*>(red + wave * 128 + (lane & 15) * 8 + (lane >> 4) * 2) = f32x2{sw16_add(u0, u2), sw16_add(u1, u3)};
    LDS_WAIT();
    unsigned t = 0;
    if (lane == 0) t = __hip_atomic_fetch_add(&ctl[C_Y], 1u, WG_RLX);
    const int tk2 = __builtin_amdgcn_readfirstlane(t);
    CBAR();
    if (tk2 == NW - 1) {
      float y0 = 0.f, y1 = 0.f;
#pragma unroll
      for (int w = 0; w < NW; ++w) { const f32x2 v = *reinterpret_cast<const f32x2*>(red + w * 128 + lane * 2); y0 += v[0]; y1 += v[1]; }
      put_g(rs, WS_YX + (sp * 8 + (lane >> 3)) * (NH * 128) + h * 128 + (lane & 7) * 16, y0, y1, tag);
    }
    return;
  }
  const int own = h * 3 + (sp - 5);
  if (own >= NOWN || tk != 0) return;
  const unsigned xv = reinterpret_cast<const unsigned*>(x)[own * 8 + (lane & 7)];
  const unsigned bv = reinterpret_cast<const unsigned*>(b2)[own * 8 + (lane & 7)];
  float g0, g1;
  {
    u32x4 v[3];
    const int off = WS_ZX + own * (NH * 128) + lane * 16;
#pragma unroll
    for (int i = 0; i < 3; ++i) v[i] = get_g(rs, off + i * 1024);
    poll<3>(rs, off, 1024, tag, v);
    float z0 = (__uint_as_float(v[0][0]) + __uint_as_float(v[1][0])) + __uint_as_float(v[2][0]);
    float z1 = (__uint_as_float(v[0][1]) + __uint_as_float(v[1][1])) + __uint_as_float(v[2][1]);
    z0 = rbf(rbf(rg_sum(z0)) + bflo(bv)); z1 = rbf(rbf(rg_sum(z1)) + bfhi(bv));
    g0 = rbf(z0 / (1.f + __expf(-z0))); g1 = rbf(z1 / (1.f + __expf(-z1)));
  }
  LDS_SPIN(LLD(&ctl[C_PUB]) != 0u);
  {
    u32x4 v[4];
    const int off = WS_YX + own * (NH * 128) + lane * 16;
#pragma unroll
    for (int i = 0; i < 4; ++i) v[i] = get_g(rs, off + i * 1024);
    poll<4>(rs, off, 1024, tag, v);
    float s0 = (__uint_as_float(v[0][0]) + __uint_as_float(v[1][0])) + (__uint_as_float(v[2][0]) + __uint_as_float(v[3][0]));
    float s1 = (__uint_as_float(v[0][1]) + __uint_as_float(v[1][1])) + (__uint_as_float(v[2][1]) + __uint_as_float(v[3][1]));
    s0 = rg_sum(s0); s1 = rg_sum(s1);
    if (lane < 8) reinterpret_cast<unsigned*>(out)[own * 8 + lane] = pk2(bflo(xv) + rbf(rbf(s0) * g0), bfhi(xv) + rbf(rbf(s1) * g1));
    if (own == 0 && lane == 0) __builtin_amdgcn_raw_buffer_store_b64(tag, rs, WS_EPOCH, 0, AUX_SC1);
  }
}

extern "C" void kernel_launch(void* const* d_in, const int* in_sizes, int n_in,
                              void* d_out, int out_size, void* d_ws, size_t ws_size,
                              hipStream_t stream) {
  const unsigned short* x = (const unsigned short*)d_in[0];
  const unsigned char* kc = (const unsigned char*)d_in[1];
  const unsigned char* vc = (const unsigned char*)d_in[2];
  const unsigned short* wq = (const unsigned short*)d_in[3];
  const unsigned short* wo = (const unsigned short*)d_in[4];
  const unsigned short* w1 = (const unsigned short*)d_in[6];
  const unsigned short* b1 = (const unsigned short*)d_in[7];
  const unsigned short* w2 = (const unsigned short*)d_in[8];
  const unsigned short* b2 = (const unsigned short*)d_in[9];
  k_main<<<NWG, NT, 0, stream>>>(kc, vc, wq, wo, w1, b1, w2, b2, x, (unsigned char*)d_ws, (unsigned short*)d_out);
}
```
